```python
import torch

def _softmax(x, axis=-1):
    return torch.softmax(x, dim=axis)

def _gelu(x):
    return torch.nn.functional.gelu(x, approximate='tanh')

def _silu(x):
    return torch.nn.functional.silu(x)

def _sigmoid(x):
    return torch.sigmoid(x)

def _layernorm(x, axis=-1, eps=1e-05):
    m = x.mean(dim=axis, keepdim=True)
    v = x.var(dim=axis, keepdim=True, unbiased=False)
    return (x - m) / torch.sqrt(v + eps)

def _scatter_add(tgt, idx, src, axis=0):
    return tgt.clone().index_add_(axis, idx, src)

def _segment_sum(src, seg, n):
    out = torch.zeros((n,) + src.shape[1:], dtype=src.dtype)
    return out.index_add_(0, seg, src)

def _pad(x, pw):
    flat = tuple((v for pair in reversed(pw) for v in pair))
    return torch.nn.functional.pad(x, flat)
kernel_name = 'adaLN_cond__linear_kernel_hedgehog__geglu_ffn__prompt'

def setup_inputs():

    _w = lambda a: (lambda t: t.to(torch.bfloat16) if t.is_floating_point() else t)(torch.as_tensor(a))

    def _A_setup():
        B, S, D, H = (1, 2048, 640, 32)
        Dh = D // H
        return {'x': _w((lambda *s: torch.randn(*s, dtype=torch.bfloat16) * 0.05)(B, S, D)), 'wqkv': _w((lambda *s: torch.randn(*s, dtype=torch.bfloat16) * 0.4)(D, 3 * D)), 'w_phi': _w((lambda *s: torch.randn(*s, dtype=torch.bfloat16) * 0.4)(H, Dh, Dh) * Dh ** (-0.5)), 'wo': _w((lambda *s: torch.randn(*s, dtype=torch.bfloat16) * 0.05)(D, D))}
    _A_in = _A_setup()

    def _B_setup():
        B, S, D, F = (1, 2048, 640, 1728)
        return {'x': _w((lambda *s: torch.randn(*s, dtype=torch.bfloat16) * 0.05)(B, S, D)), 'w_gate': _w((lambda *s: torch.randn(*s, dtype=torch.bfloat16) * 0.05)(D, F)), 'w_up': _w((lambda *s: torch.randn(*s, dtype=torch.bfloat16) * 0.05)(D, F)), 'w_down': _w((lambda *s: torch.randn(*s, dtype=torch.bfloat16) * 0.05)(F, D))}
    _B_in = _B_setup()
    return {'A_x': _A_in['x'], 'A_wqkv': _A_in['wqkv'], 'A_w_phi': _A_in['w_phi'], 'A_wo': _A_in['wo'], 'B_x': _B_in['x'], 'B_w_gate': _B_in['w_gate'], 'B_w_up': _B_in['w_up'], 'B_w_down': _B_in['w_down'], 'c': _w((lambda *s: torch.randn(*s, dtype=torch.bfloat16) * 0.1)(1, 640) * 8.0), 'w_mod': _w((lambda *s: torch.randn(*s, dtype=torch.bfloat16) * 0.1)(640, 6 * 640) * 640 ** (-0.5) * 8.0)}

def reference(A_x, A_wqkv, A_w_phi, A_wo, B_x, B_w_gate, B_w_up, B_w_down, c, w_mod):

    def _A_ref(x, wqkv, w_phi, wo):
        B, S, D = x.shape
        H = 32
        Dh = D // H
        qkv = (lambda x, *d: x.permute(*d))((x @ wqkv).reshape(B, S, 3, H, Dh), 2, 0, 3, 1, 4)
        q, k, v = (qkv[0], qkv[1], qkv[2])
        pq = torch.einsum('bhsd,hdf->bhsf', q, w_phi)
        pk = torch.einsum('bhsd,hdf->bhsf', k, w_phi)
        phi_q = torch.concatenate([_softmax(pq, axis=-1), _softmax(-pq, axis=-1)], axis=-1)
        phi_k = torch.concatenate([_softmax(pk, axis=-1), _softmax(-pk, axis=-1)], axis=-1)
        kv = torch.einsum('bhsf,bhsd->bhfd', phi_k, v)
        z = torch.einsum('bhsf->bhf', phi_k)
        num = torch.einsum('bhqf,bhfd->bhqd', phi_q, kv)
        den = torch.einsum('bhqf,bhf->bhq', phi_q, z)[..., None] + 1e-06
        o = num / den
        return (lambda x, *d: x.permute(*d))(o, 0, 2, 1, 3).reshape(B, S, D) @ wo

    def _B_ref(x, w_gate, w_up, w_down):
        return _gelu(x @ w_gate) * (x @ w_up) @ w_down
    x = A_x
    mod = (_silu(c) @ w_mod).reshape(c.shape[0], 6, 1, -1)
    sa, ca, ga, sb, cb, gb = (mod[:, 0].clone(), mod[:, 1].clone(), mod[:, 2].clone(), mod[:, 3].clone(), mod[:, 4].clone(), mod[:, 5].clone())
    h = _layernorm(x, axis=-1) * (1 + ca) + sa
    x = x + ga * _A_ref(h, A_wqkv, A_w_phi, A_wo)
    h = _layernorm(x, axis=-1) * (1 + cb) + sb
    x = x + gb * _B_ref(h, B_w_gate, B_w_up, B_w_down)
    return x
```

```cpp
#include <hip/hip_runtime.h>
#include <hip/hip_bf16.h>
typedef __hip_bfloat16 bf16;
#define S_ 2048
#define D_ 640
#define H_ 32
#define DH 20
#define F_ 1728
__device__ __forceinline__ float b2f(bf16 v){return __bfloat162float(v);}
__device__ __forceinline__ float rb(float v){return __bfloat162float(__float2bfloat16(v));}

__global__ void k_mod(const bf16* c,const bf16* wmod,float* mod){
  int n=blockIdx.x*blockDim.x+threadIdx.x; if(n>=6*D_)return;
  float a=0; for(int k=0;k<D_;k++){float cv=b2f(c[k]); float s=rb(cv/(1.f+__expf(-cv))); a+=s*b2f(wmod[k*6*D_+n]);}
  mod[n]=rb(a);
}
__global__ void k_ln(const float* xin,const float* shift,const float* scale,float* h){
  int s=blockIdx.x; __shared__ float red[256];
  int t=threadIdx.x; float a=0; for(int k=t;k<D_;k+=256)a+=xin[s*D_+k];
  red[t]=a; __syncthreads(); for(int o=128;o>0;o>>=1){if(t<o)red[t]+=red[t+o];__syncthreads();}
  float m=red[0]/D_; __syncthreads();
  a=0; for(int k=t;k<D_;k+=256){float d=xin[s*D_+k]-m;a+=d*d;}
  red[t]=a; __syncthreads(); for(int o=128;o>0;o>>=1){if(t<o)red[t]+=red[t+o];__syncthreads();}
  float v=red[0]/D_; float r=1.f/sqrtf(v+1e-5f);
  for(int k=t;k<D_;k+=256) h[s*D_+k]=rb((xin[s*D_+k]-m)*r*(1.f+scale[k])+shift[k]);
}
__global__ void k_cvt(const bf16* x,float* y,int n){int i=blockIdx.x*blockDim.x+threadIdx.x; if(i<n)y[i]=b2f(x[i]);}
__global__ void k_gemm(const float* A,const bf16* W,float* C,int M,int N,int K){
  __shared__ float As[16][17],Bs[16][17];
  int tx=threadIdx.x,ty=threadIdx.y; int r=blockIdx.y*16+ty,c=blockIdx.x*16+tx; float a=0;
  for(int k0=0;k0<K;k0+=16){As[ty][tx]=A[r*K+k0+tx];Bs[ty][tx]=b2f(W[(k0+ty)*N+c]);__syncthreads();
    for(int k=0;k<16;k++)a+=As[ty][k]*Bs[k][tx]; __syncthreads();}
  C[r*N+c]=rb(a);
}
__global__ void k_phi(const float* qkv,const bf16* wphi,float* phi){
  int i=blockIdx.x*blockDim.x+threadIdx.x; if(i>=2*S_*H_)return;
  int h=i%H_, s=(i/H_)%S_, w=i/(H_*S_);
  float mx=-1e30f,mn=1e30f;
  float* o=phi+(((size_t)w*S_+s)*H_+h)*40;
  for(int f=0;f<DH;f++){float a=0; for(int d=0;d<DH;d++)a+=qkv[s*3*D_+w*D_+h*DH+d]*b2f(wphi[h*DH*DH+d*DH+f]); a=rb(a); o[f]=a; mx=fmaxf(mx,a); mn=fminf(mn,a);}
  float s1=0,s2=0;
  for(int f=0;f<DH;f++){s1+=__expf(o[f]-mx); s2+=__expf(mn-o[f]);}
  for(int f=0;f<DH;f++){float p=o[f]; o[f]=rb(__expf(p-mx)/s1); o[20+f]=rb(__expf(mn-p)/s2);}
}
__global__ void k_kv(const float* qkv,const float* phi,float* kv){
  int i=blockIdx.x*blockDim.x+threadIdx.x; if(i>=H_*40*21)return;
  int d=i%21,f=(i/21)%40,h=i/(21*40); float a=0;
  for(int s=0;s<S_;s++){float pk=phi[(((size_t)S_+s)*H_+h)*40+f]; a+=pk*(d<20?qkv[s*3*D_+2*D_+h*DH+d]:1.f);}
  kv[i]=rb(a);
}
__global__ void k_o(const float* phi,const float* kv,float* o){
  int i=blockIdx.x*blockDim.x+threadIdx.x; if(i>=S_*D_)return;
  int d=i%DH,h=(i/DH)%H_,s=i/D_; float n=0,de=0;
  for(int f=0;f<40;f++){float pq=phi[((size_t)s*H_+h)*40+f]; n+=pq*kv[(h*40+f)*21+d]; de+=pq*kv[(h*40+f)*21+20];}
  o[i]=rb(rb(n)/rb(rb(de)+1e-6f));
}
__global__ void k_res(const float* x,const float* g,const float* y,float* o,bf16* ob){
  int i=blockIdx.x*blockDim.x+threadIdx.x; if(i>=S_*D_)return;
  float v=rb(x[i]+rb(g[i%D_]*y[i])); if(o)o[i]=v; if(ob)ob[i]=__float2bfloat16(v);
}
__global__ void k_geglu(const float* g,const float* u,float* a){
  int i=blockIdx.x*blockDim.x+threadIdx.x; if(i>=S_*F_)return;
  float x=g[i]; float t=tanhf(0.7978845608f*(x+0.044715f*x*x*x)); a[i]=rb(rb(0.5f*x*(1.f+t))*u[i]);
}
extern "C" void kernel_launch(void* const* d_in, const int* in_sizes, int n_in,
                              void* d_out, int out_size, void* d_ws, size_t ws_size,
                              hipStream_t stream) {
  const bf16* x=(const bf16*)d_in[0]; const bf16* wqkv=(const bf16*)d_in[1]; const bf16* wphi=(const bf16*)d_in[2];
  const bf16* wo=(const bf16*)d_in[3]; const bf16* wg=(const bf16*)d_in[5]; const bf16* wu=(const bf16*)d_in[6];
  const bf16* wd=(const bf16*)d_in[7]; const bf16* c=(const bf16*)d_in[8]; const bf16* wmod=(const bf16*)d_in[9];
  float* ws=(float*)d_ws;
  float* mod=ws; ws+=4096; float* xf=ws; ws+=S_*D_; float* h=ws; ws+=S_*D_; float* qkv=ws; ws+=S_*3*D_;
  float* phi=ws; ws+=2*S_*H_*40; float* kv=ws; ws+=32768; float* o=ws; ws+=S_*D_; float* at=ws; ws+=S_*D_;
  float* x1=ws; ws+=S_*D_; float* g=ws; ws+=S_*F_; float* u=ws; ws+=S_*F_; float* a=ws; ws+=S_*F_;
  k_mod<<<15,256,0,stream>>>(c,wmod,mod);
  k_cvt<<<S_*D_/256,256,0,stream>>>(x,xf,S_*D_);
  k_ln<<<S_,256,0,stream>>>(xf,mod,mod+D_,h);
  k_gemm<<<dim3(3*D_/16,S_/16),dim3(16,16),0,stream>>>(h,wqkv,qkv,S_,3*D_,D_);
  k_phi<<<2*S_*H_/256,256,0,stream>>>(qkv,wphi,phi);
  k_kv<<<(H_*40*21+63)/64,64,0,stream>>>(qkv,phi,kv);
  k_o<<<S_*D_/256,256,0,stream>>>(phi,kv,o);
  k_gemm<<<dim3(D_/16,S_/16),dim3(16,16),0,stream>>>(o,wo,at,S_,D_,D_);
  k_res<<<S_*D_/256,256,0,stream>>>(xf,mod+2*D_,at,x1,nullptr);
  k_ln<<<S_,256,0,stream>>>(x1,mod+3*D_,mod+4*D_,h);
  k_gemm<<<dim3(F_/16,S_/16),dim3(16,16),0,stream>>>(h,wg,g,S_,F_,D_);
  k_gemm<<<dim3(F_/16,S_/16),dim3(16,16),0,stream>>>(h,wu,u,S_,F_,D_);
  k_geglu<<<S_*F_/256,256,0,stream>>>(g,u,a);
  k_gemm<<<dim3(D_/16,S_/16),dim3(16,16),0,stream>>>(a,wd,at,S_,D_,F_);
  k_res<<<S_*D_/256,256,0,stream>>>(x1,mod+5*D_,at,nullptr,(bf16*)d_out);
}
```

```cpp
#include <hip/hip_runtime.h>
#include <hip/hip_bf16.h>
#include <utility>

typedef unsigned short u16;
typedef unsigned v4u __attribute__((ext_vector_type(4)));
typedef unsigned v2u __attribute__((ext_vector_type(2)));
typedef float f32x2 __attribute__((ext_vector_type(2)));
typedef float f32x4 __attribute__((ext_vector_type(4)));
typedef float f32x16 __attribute__((ext_vector_type(16)));
typedef __bf16 bf16x8 __attribute__((ext_vector_type(8)));
typedef __bf16 bf16x2 __attribute__((ext_vector_type(2)));
typedef short s16x4 __attribute__((ext_vector_type(4)));
#define LAS __attribute__((address_space(3)))
#define GAS __attribute__((address_space(1)))
#define DI __device__ __forceinline__
#define RLX_AGENT __ATOMIC_RELAXED, __HIP_MEMORY_SCOPE_AGENT

constexpr int S_ = 2048, D_ = 640, F_ = 1728;

constexpr int C_KV = 0, C_CSB = 1, C_O = 2, C_X = 3, C_A1 = 4, C_A2 = 5, NCNT = 6;
constexpr int CNT_OFF = 0;
constexpr int TMO_OFF = NCNT * 256 * 256;
constexpr int MODV_OFF = 0x80000;
constexpr int SAB_OFF = MODV_OFF + 16384;
constexpr int CBP_OFF = MODV_OFF + 20480;
constexpr int XH_OFF = MODV_OFF + 32768;
constexpr int WP_OFF = XH_OFF + 64 * 40 * 1024;
constexpr int WO_OFF = WP_OFF + 32 * 80 * 1024;
constexpr int WGU_OFF = WO_OFF + 8 * 100 * 1024;
constexpr int WD_OFF = WGU_OFF + 16 * 280 * 1024;
constexpr int KVP_OFF = WD_OFF + 8 * 270 * 1024;
constexpr int CSB_OFF = KVP_OFF + 32 * 8 * 5 * 1024;
constexpr int OT_OFF = CSB_OFF + 16 * 8 * 7 * 256;
constexpr int XS_OFF = OT_OFF + 128 * 20 * 1024;
constexpr int ST_OFF = XS_OFF + 64 * 40 * 1024;
constexpr int ACT_OFF = ST_OFF + 8 * 2048 * 8;
constexpr int DBG_OFF = ACT_OFF + 128 * 54 * 1024;
constexpr int WS_BYTES = DBG_OFF + 256 * 32 * 8;

DI float bf2f(unsigned b) { return __builtin_bit_cast(float, b << 16); }
DI float bflo(unsigned p) { return __builtin_bit_cast(float, p << 16); }
DI float bfhi(unsigned p) { return __builtin_bit_cast(float, p & 0xffff0000u); }
DI unsigned pk2(float a, float b) { f32x2 v = {a, b}; return __builtin_bit_cast(unsigned, __builtin_convertvector(v, bf16x2)); }
DI float rbf(float a) { return bflo(pk2(a, a)); }
DI f32x16 mfma32(v4u a, v4u b, f32x16 c) { return __builtin_amdgcn_mfma_f32_32x32x16_bf16(__builtin_bit_cast(bf16x8, a), __builtin_bit_cast(bf16x8, b), c, 0, 0, 0); }
DI f32x4 mfma16(v4u a, v4u b, f32x4 c) { return __builtin_amdgcn_mfma_f32_16x16x32_bf16(__builtin_bit_cast(bf16x8, a), __builtin_bit_cast(bf16x8, b), c, 0, 0, 0); }
DI v2u trread(LAS char* p) { return __builtin_bit_cast(v2u, __builtin_amdgcn_ds_read_tr16_b64_v4i16((LAS s16x4*)p)); }
DI float dot2(unsigned a, unsigned b, float c) { return __builtin_amdgcn_fdot2_f32_bf16(__builtin_bit_cast(bf16x2, a), __builtin_bit_cast(bf16x2, b), c, false); }

DI int wo_row(int ks, int g, int j) {
  int head, d;
  if (ks < 16) { head = 2 * ks + (g >> 1); d = 8 * (g & 1) + j; }
  else { head = 8 * (ks - 16) + 2 * g + (j >> 2); d = 16 + (j & 3); }
  return 20 * head + d;
}
DI int xs_col(int s, int half, int j) { return s < 4 ? 20 * s + 8 * half + j : 20 * (2 * half + (j >> 2)) + 16 + (j & 3); }
DI int act_row(int id, int j) {
  if (id < 128) return 108 * (id >> 3) + 8 * (id & 7) + j;
  int u = id - 128, pr = u / 11, v = u - 11 * pr;
  if (v < 10) { int o = v >= 5; return 108 * (2 * pr + o) + 64 + 8 * (v - 5 * o) + j; }
  return 108 * (2 * pr + (j >> 2)) + 104 + (j & 3);
}
__host__ __device__ constexpr int gu_idx(int k16, int ft) { return ft < 3 ? k16 * 3 + ft : 120 + (ft - 3) * 40 + k16; }

template <int... I, class F> DI void sfor_impl(std::integer_sequence<int, I...>, F&& f) { (f(std::integral_constant<int, I>{}), ...); }
template <int N, class F> DI void sfor(F&& f) { sfor_impl(std::make_integer_sequence<int, N>{}, f); }
#define SLAM(i) [&](auto i##_c) __attribute__((always_inline))
constexpr int J_MOD = 0, N_MOD = 120;
constexpr int J_GU = J_MOD + N_MOD, N_GU = 128;
constexpr int J_XH = J_GU + N_GU, N_XH = 128;
constexpr int J_FOLD = J_XH + N_XH, N_FOLD = 160;
constexpr int J_WD = J_FOLD + N_FOLD, N_WD = 216;
constexpr int J_WO = J_WD + N_WD, N_WO = 80;
constexpr int K1_GRID = J_WO + N_WO;
constexpr int K1_LDS = 2 * 80 * 240;
static_assert(J_FOLD % 8 == 0, "fold jobs keep their XCD label");

DI void job_mod(int j, const u16* c, const u16* wmod, char* ws, LAS char* L) {
  const int t = threadIdx.x, q = t & 3, r0 = t >> 2;
  v4u w[10]; unsigned cv[10];
#pragma unroll
  for (int i = 0; i < 10; ++i) { w[i] = *(const v4u*)(wmod + (size_t)(r0 + 64 * i) * 3840 + 32 * j + 8 * q); cv[i] = c[r0 + 64 * i]; }
  float a[8] = {0.f, 0.f, 0.f, 0.f, 0.f, 0.f, 0.f, 0.f};
#pragma unroll
  for (int i = 0; i < 10; ++i) {
    const float x = bf2f(cv[i]);
    const float s = rbf(x * __builtin_amdgcn_rcpf(1.f + __builtin_amdgcn_exp2f(-1.4426950408889634f * x)));
    a[0] += s * bflo(w[i].x); a[1] += s * bfhi(w[i].x); a[2] += s * bflo(w[i].y); a[3] += s * bfhi(w[i].y);
    a[4] += s * bflo(w[i].z); a[5] += s * bfhi(w[i].z); a[6] += s * bflo(w[i].w); a[7] += s * bfhi(w[i].w);
  }
#pragma unroll
  for (int m = 4; m <= 32; m <<= 1)
#pragma unroll
    for (int e = 0; e < 8; ++e) a[e] += __shfl_xor(a[e], m);
  LAS float* red = (LAS float*)L;
  if ((t & 63) < 4) {
#pragma unroll
    for (int e = 0; e < 8; ++e) red[(t >> 6) * 32 + 8 * q + e] = a[e];
  }
  __syncthreads();
  if (t < 32) {
    float v = rbf((red[t] + red[32 + t]) + (red[64 + t] + red[96 + t]));
    const int n = 32 * j + t, which = n / 640, k = n - 640 * which;
    if (which == 1 || which == 4) v = rbf(1.f + v);
    ((float*)(ws + MODV_OFF))[n] = v;
    const u16 vb = (u16)(__builtin_bit_cast(unsigned, v) >> 16);
    if (which == 0) ((u16*)(ws + SAB_OFF))[k] = vb;
    if (which == 3 || which == 4) {
      const int cg = k / 80, l = k - 80 * cg, g = l / 20, i = l - 20 * g;
      const int slot = i < 16 ? 16 * (5 * cg + g) + i : 16 * (5 * cg + 4) + 4 * g + (i - 16);
      ((u16*)(ws + CBP_OFF))[(which == 4 ? 0 : 640) + slot] = vb;
    }
  }
}

DI void job_xhat(int j, const u16* x, char* ws, LAS char* L) {
  const int t = threadIdx.x, row = t >> 4, i = t & 15;
  v4u v[5];
#pragma unroll
  for (int jj = 0; jj < 5; ++jj) v[jj] = *(const v4u*)(x + (size_t)(16 * j + row) * 640 + 8 * (i + 16 * jj));
  float f[40];
#pragma unroll
  for (int jj = 0; jj < 5; ++jj) {
    f[8 * jj + 0] = bflo(v[jj].x); f[8 * jj + 1] = bfhi(v[jj].x); f[8 * jj + 2] = bflo(v[jj].y); f[8 * jj + 3] = bfhi(v[jj].y);
    f[8 * jj + 4] = bflo(v[jj].z); f[8 * jj + 5] = bfhi(v[jj].z); f[8 * jj + 6] = bflo(v[jj].w); f[8 * jj + 7] = bfhi(v[jj].w);
  }
  float s = 0.f;
#pragma unroll
  for (int e = 0; e < 40; ++e) s += f[e];
#pragma unroll
  for (int m = 1; m <= 8; m <<= 1) s += __shfl_xor(s, m);
  const float mean = s * (1.f / 640.f);
  float q = 0.f;
#pragma unroll
  for (int e = 0; e < 40; ++e) { const float d = f[e] - mean; q += d * d; }
#pragma unroll
  for (int m = 1; m <= 8; m <<= 1) q += __shfl_xor(q, m);
  const float mb = rbf(mean);
  const float rs = 1.f / rbf(sqrtf(rbf(rbf(q * (1.f / 640.f)) + 1e-5f)));
#pragma unroll
  for (int jj = 0; jj < 5; ++jj) {
    v4u o;
    o.x = pk2(rbf(f[8 * jj + 0] - mb) * rs, rbf(f[8 * jj + 1] - mb) * rs);
    o.y = pk2(rbf(f[8 * jj + 2] - mb) * rs, rbf(f[8 * jj + 3] - mb) * rs);
    o.z = pk2(rbf(f[8 * jj + 4] - mb) * rs, rbf(f[8 * jj + 5] - mb) * rs);
    o.w = pk2(rbf(f[8 * jj + 6] - mb) * rs, rbf(f[8 * jj + 7] - mb) * rs);
    *(LAS v4u*)(L + (i + 16 * jj) * 272 + row * 16) = o;
  }
  __syncthreads();
  char* dst = ws + XH_OFF + (size_t)(j >> 1) * 40960 + (j & 1) * 256 + i * 16;
#pragma unroll
  for (int it = 0; it < 5; ++it) {
    const int qq = row + 16 * it;
    *(v4u*)(dst + qq * 512) = *(LAS v4u*)(L + qq * 272 + i * 16);
  }
}

DI void job_fold(int j, const u16* wqkv, const u16* wphi, char* ws, LAS char* L) {
  const int t = threadIdx.x, lane = t & 63, w = t >> 6, hg = j & 7, kb = j >> 3;
  constexpr int SR = 176, PHI = 3 * 32 * SR;
  v4u v[4];
#pragma unroll
  for (int it = 0; it < 4; ++it) {
    int idx = t + 256 * it; if (idx >= 960) idx = 959;
    const int type = idx / 320, rem = idx - 320 * type, row = rem / 10, ch = rem - 10 * row;
    v[it] = *(const v4u*)(wqkv + (size_t)(32 * kb + row) * 1920 + type * 640 + 80 * hg + 8 * ch);
  }
  const v4u pv = *(const v4u*)(wphi + 1600 * hg + 8 * (t < 200 ? t : 199));
#pragma unroll
  for (int it = 0; it < 4; ++it) {
    int idx = t + 256 * it; if (idx >= 960) idx = 959;
    const int type = idx / 320, rem = idx - 320 * type, row = rem / 10, ch = rem - 10 * row;
    *(LAS v4u*)(L + (type * 32 + row) * SR + ch * 16) = v[it];
  }
  if (t < 200) *(LAS v4u*)(L + PHI + t * 16) = pv;
  __syncthreads();
  const int rho = lane & 31, hh = lane >> 5, hf = (rho >> 2) & 1, r = (rho & 3) + 4 * (rho >> 3);
  const int krow = 16 * (r >> 3) + 8 * hf + (r & 7);
  v4u A[3][2];
#pragma unroll
  for (int ty = 0; ty < 3; ++ty) {
    LAS char* b = L + (ty * 32 + krow) * SR + 40 * w;
    const v2u a0 = *(LAS v2u*)(b + 16 * hh), a1 = *(LAS v2u*)(b + 16 * hh + 8), a2 = *(LAS v2u*)(b + 32);
    A[ty][0] = v4u{a0.x, a0.y, a1.x, a1.y};
    A[ty][1] = v4u{a2.x, a2.y, a2.x, a2.y};
  }
  LAS u16* ph = (LAS u16*)(L + PHI) + 400 * w;
  f32x16 acc[2];
#pragma unroll
  for (int ft = 0; ft < 2; ++ft) {
    const int sg = 16 * ft + r;
    const bool isp = ft == 0 ? sg < 10 : sg >= 22;
    const int f = isp ? 10 * hf + (ft == 0 ? sg : sg - 22) : 0;
    const int rp = 12 * hf + sg - 10;
    const int dv = (isp || rp >= 20) ? -1 : (rp < 4 ? rp : rp < 8 ? rp + 4 : rp < 12 ? rp - 4 : rp);
    unsigned e[12];
#pragma unroll
    for (int jj = 0; jj < 8; ++jj) e[jj] = ph[(8 * hh + jj) * 20 + f];
#pragma unroll
    for (int jj = 0; jj < 4; ++jj) e[8 + jj] = ph[(16 + jj) * 20 + f];
    v4u P0 = {e[0] | (e[1] << 16), e[2] | (e[3] << 16), e[4] | (e[5] << 16), e[6] | (e[7] << 16)};
    v4u P1 = {e[8] | (e[9] << 16), e[10] | (e[11] << 16), 0u, 0u};
    if (!isp) P0 = v4u{0u, 0u, 0u, 0u};
    if (!isp || hh) P1 = v4u{0u, 0u, 0u, 0u};
    v4u V[2];
#pragma unroll
    for (int s = 0; s < 2; ++s) {
      const int rel = dv - 16 * s - 8 * hh;
      const unsigned one = (rel & 1) ? 0x3F800000u : 0x00003F80u;
      const bool in = dv >= 0 && rel >= 0 && rel < 8;
      V[s] = v4u{(in && (rel >> 1) == 0) ? one : 0u, (in && (rel >> 1) == 1) ? one : 0u, (in && (rel >> 1) == 2) ? one : 0u, (in && (rel >> 1) == 3) ? one : 0u};
    }
    f32x16 a = {0.f, 0.f, 0.f, 0.f, 0.f, 0.f, 0.f, 0.f, 0.f, 0.f, 0.f, 0.f, 0.f, 0.f, 0.f, 0.f};
    const int ty = ft == 0 ? 1 : 0;
    a = mfma32(A[ty][0], P0, a); a = mfma32(A[ty][1], P1, a);
    a = mfma32(A[2][0], V[0], a); a = mfma32(A[2][1], V[1], a);
    acc[ft] = a;
  }
  char* dst = ws + WP_OFF + (size_t)(4 * hg + w) * 81920 + (size_t)(2 * kb) * 2048 + lane * 16;
#pragma unroll
  for (int ft = 0; ft < 2; ++ft)
#pragma unroll
    for (int s = 0; s < 2; ++s)
      *(v4u*)(dst + s * 2048 + ft * 1024) = v4u{pk2(acc[ft][8 * s], acc[ft][8 * s + 1]), pk2(acc[ft][8 * s + 2], acc[ft][8 * s + 3]),
                                                pk2(acc[ft][8 * s + 4], acc[ft][8 * s + 5]), pk2(acc[ft][8 * s + 6], acc[ft][8 * s + 7])};
}

DI void job_gu(int j, const u16* wg, const u16* wu, char* ws, LAS char* L) {
  const int t = threadIdx.x, lane = t & 63, w = t >> 6, fg = j & 15, cg = j >> 4;
  const int c0 = 108 * fg - 4 * (fg & 1);
  v4u v[9];
#pragma unroll
  for (int it = 0; it < 9; ++it) {
    int idx = t + 256 * it; if (idx >= 2240) idx = 2239;
    const int rr = idx / 14, ch = idx - 14 * rr, mat = rr >= 80, row = rr - 80 * mat;
    v[it] = *(const v4u*)((mat ? wu : wg) + (size_t)(80 * cg + row) * F_ + c0 + 8 * ch);
  }
#pragma unroll
  for (int it = 0; it < 9; ++it) {
    int idx = t + 256 * it; if (idx >= 2240) idx = 2239;
    const int rr = idx / 14, ch = idx - 14 * rr;
    *(LAS v4u*)(L + rr * 240 + ch * 16) = v[it];
  }
  __syncthreads();
  const int gam = lane >> 4, half = gam >> 1, mat = gam & 1, q = (lane >> 2) & 3, p = lane & 3;
  LAS char* base = L + mat * 80 * 240 + (8 * (p & 1) + 4 * (p >> 1) + 4 * (fg & 1)) * 2;
  char* dst = ws + WGU_OFF + (size_t)fg * 286720 + lane * 16;
  for (int fr = w; fr < 35; fr += 4) {
    const int s = fr / 7, ft = fr - 7 * s;
    const v2u a = trread(base + xs_col(s, half, q) * 240 + ft * 32);
    const v2u b = trread(base + xs_col(s, half, 4 + q) * 240 + ft * 32);
    v4u o = {a.x, a.y, b.x, b.y};
    if (ft == 6 && q == 3) o = v4u{0u, 0u, 0u, 0u};
    *(v4u*)(dst + (size_t)gu_idx(5 * cg + s, ft) * 1024) = o;
  }
}

template <bool DOWN> DI void job_t16(int j, const u16* wsrc, char* ws, LAS char* L) {
  const int t = threadIdx.x, lane = t & 63, w = t >> 6, cg = j & 7, ks0 = 2 * (j >> 3);
  v4u v[3];
#pragma unroll
  for (int it = 0; it < 3; ++it) {
    int idx = t + 256 * it; if (idx >= 640) idx = 639;
    const int slot = idx / 10, ch = idx - 10 * slot;
    const int row = DOWN ? act_row(4 * ks0 + (slot >> 3), slot & 7) : wo_row(ks0 + (slot >> 5), (slot >> 3) & 3, slot & 7);
    v[it] = *(const v4u*)(wsrc + (size_t)row * 640 + 80 * cg + 8 * ch);
  }
#pragma unroll
  for (int it = 0; it < 3; ++it) {
    int idx = t + 256 * it; if (idx >= 640) idx = 639;
    const int slot = idx / 10, ch = idx - 10 * slot;
    *(LAS v4u*)(L + slot * 176 + ch * 16) = v[it];
  }
  __syncthreads();
  const int g = lane >> 4, q = (lane >> 2) & 3, p = lane & 3;
  LAS char* base = L + (8 * g + q) * 176 + 40 * p;
  char* dst = ws + (DOWN ? WD_OFF + (size_t)cg * 276480 : WO_OFF + (size_t)cg * 102400) + (size_t)ks0 * 5120 + lane * 16;
  for (int fr = w; fr < 10; fr += 4) {
    const int ksl = fr >= 5, ct = fr - 5 * ksl;
    const v2u a = trread(base + ksl * 32 * 176 + ct * 8);
    const v2u b = trread(base + (ksl * 32 + 4) * 176 + ct * 8);
    *(v4u*)(dst + fr * 1024) = v4u{a.x, a.y, b.x, b.y};
  }
}

__global__ __launch_bounds__(256) void k_prep(const u16* x, const u16* wqkv, const u16* wphi, const u16* wo, const u16* wg,
                                              const u16* wu, const u16* wd, const u16* c, const u16* wmod, char* ws) {
  __shared__ __attribute__((aligned(16))) char smem[K1_LDS];
  LAS char* L = (LAS char*)smem;
  const int b = blockIdx.x;
  if (b < NCNT) *(unsigned*)(ws + CNT_OFF + (b * 256 + threadIdx.x) * 256) = 0u;
  if (b == NCNT && threadIdx.x == 0) *(unsigned*)(ws + TMO_OFF) = 0u;
  if (b < J_GU) job_mod(b - J_MOD, c, wmod, ws, L);
  else if (b < J_XH) job_gu(b - J_GU, wg, wu, ws, L);
  else if (b < J_FOLD) job_xhat(b - J_XH, x, ws, L);
  else if (b < J_WD) job_fold(b - J_FOLD, wqkv, wphi, ws, L);
  else if (b < J_WO) job_t16<true>(b - J_WD, wd, ws, L);
  else job_t16<false>(b - J_WO, wo, ws, L);
}
typedef __amdgpu_buffer_rsrc_t rsrc_t;
constexpr int SLOT = 36 * 1024, MISC = 4 * SLOT;
constexpr int L_TB = 81920, TB_ROW = 200, TB_SZ = 32 * TB_ROW;
constexpr int L_KVR = 3 * SLOT;
constexpr int L_BIASP = MISC, L_BIAST = MISC + 2048, L_MODST = MISC + 3072, L_KVA = MISC + 7168, L_CSB = MISC + 10240, L_FLAG = MISC + 12288;
constexpr int MAIN_LDS = 163840;
constexpr float LOG2E = 1.4426950408889634f;
constexpr unsigned SPIN_CAP = 1u << 16;

DI v4u ldp(rsrc_t rs, int voff, int soff) { return __builtin_amdgcn_raw_buffer_load_b128(rs, voff, soff, 0); }
DI v4u ldc(rsrc_t rs, int voff, int soff) { return __builtin_amdgcn_raw_buffer_load_b128(rs, voff, soff, 16); }
DI void stc(rsrc_t rs, int voff, int soff, v4u v) { __builtin_amdgcn_raw_buffer_store_b128(v, rs, voff, soff, 16); }
DI void stc2(rsrc_t rs, int voff, int soff, v2u v) { __builtin_amdgcn_raw_buffer_store_b64(v, rs, voff, soff, 16); }
#define DRAIN() asm volatile("s_waitcnt vmcnt(0)" ::: "memory")
#define BAR() asm volatile("s_waitcnt lgkmcnt(0)\n\ts_barrier" ::: "memory")
#define BAR_RAW() asm volatile("s_barrier" ::: "memory")
#define SCHED() __builtin_amdgcn_sched_barrier(0)
#define CFENCE() asm volatile("" ::: "memory")
template <int N> DI void wait_vm() { asm volatile("s_waitcnt vmcnt(%0)" ::"i"(N) : "memory"); }

DI void dma1(rsrc_t rs, int v16, int soff, int lds) {
  asm volatile("s_nop 4\n\ts_mov_b32 m0, %0\n\ts_nop 0\n\tbuffer_load_dwordx4 %1, %2, %3 offen lds" ::"s"(lds), "v"(v16), "s"(rs), "s"(soff) : "memory");
}
DI void dma4(rsrc_t rs, int v16, int soff, int lds) {
  asm volatile("s_nop 4\n\ts_mov_b32 m0, %0\n\ts_nop 0\n\t"
               "buffer_load_dwordx4 %1, %2, %3 offen lds\n\tbuffer_load_dwordx4 %1, %2, %3 offen offset:1024 lds\n\t"
               "buffer_load_dwordx4 %1, %2, %3 offen offset:2048 lds\n\tbuffer_load_dwordx4 %1, %2, %3 offen offset:3072 lds" ::"s"(lds), "v"(v16), "s"(rs), "s"(soff) : "memory");
}
template <int N> DI void dmaN(rsrc_t rs, int v16, int soff, int lds) {
#pragma unroll
  for (int i = 0; i + 4 <= N; i += 4) dma4(rs, v16, soff + i * 1024, lds + i * 1024);
#pragma unroll
  for (int i = N & ~3; i < N; ++i) dma1(rs, v16, soff + i * 1024, lds + i * 1024);
}

DI GAS unsigned* cnt_ptr(char* ws, int type, int id) { return (GAS unsigned*)(ws + CNT_OFF + (type * 256 + id) * 256); }
DI void poll_ge(char* ws, int type, int id, unsigned want) {
  GAS unsigned* p = cnt_ptr(ws, type, id);
  GAS unsigned* tmo = (GAS unsigned*)(ws + TMO_OFF);
  for (unsigned spins = 0;;) {
    if (__builtin_amdgcn_readfirstlane(__hip_atomic_load(p, RLX_AGENT)) >= want) break;
    ++spins;
    if ((spins & 255u) == 0u) {
      if (__builtin_amdgcn_readfirstlane(__hip_atomic_load(tmo, RLX_AGENT)) != 0u) break;
      if (spins > SPIN_CAP) { __hip_atomic_store(tmo, 1u, RLX_AGENT); break; }
    }
  }
  __builtin_amdgcn_fence(__ATOMIC_ACQUIRE, "wavefront");
}
DI void signal(char* ws, int type, int id) { (void)__hip_atomic_fetch_add(cnt_ptr(ws, type, id), 1u, RLX_AGENT); }

DI float swapmax(float v) { auto r = __builtin_amdgcn_permlane32_swap(__builtin_bit_cast(unsigned, v), __builtin_bit_cast(unsigned, v), false, false); return fmaxf(__uint_as_float(r[0]), __uint_as_float(r[1])); }
DI float swapmin(float v) { auto r = __builtin_amdgcn_permlane32_swap(__builtin_bit_cast(unsigned, v), __builtin_bit_cast(unsigned, v), false, false); return fminf(__uint_as_float(r[0]), __uint_as_float(r[1])); }
DI float swapadd(float v) { auto r = __builtin_amdgcn_permlane32_swap(__builtin_bit_cast(unsigned, v), __builtin_bit_cast(unsigned, v), false, false); return __uint_as_float(r[0]) + __uint_as_float(r[1]); }
DI float swap16add(float v) { auto r = __builtin_amdgcn_permlane16_swap(__builtin_bit_cast(unsigned, v), __builtin_bit_cast(unsigned, v), false, false); return __uint_as_float(r[0]) + __uint_as_float(r[1]); }

DI void softpair(const float (&p)[10], unsigned (&o)[10]) {
  float mx = fmaxf(fmaxf(fmaxf(p[0], p[1]), fmaxf(p[2], p[3])), fmaxf(fmaxf(p[4], p[5]), fmaxf(p[6], p[7])));
  float mn = fminf(fminf(fminf(p[0], p[1]), fminf(p[2], p[3])), fminf(fminf(p[4], p[5]), fminf(p[6], p[7])));
  mx = swapmax(fmaxf(mx, fmaxf(p[8], p[9])));
  mn = swapmin(fminf(mn, fminf(p[8], p[9])));
  float a[10], b[10], sa = 0.f, sb = 0.f;
#pragma unroll
  for (int i = 0; i < 10; ++i) { a[i] = __builtin_amdgcn_exp2f(p[i] - mx); b[i] = __builtin_amdgcn_exp2f(mn - p[i]); sa += a[i]; sb += b[i]; }
  const float ra = __builtin_amdgcn_rcpf(swapadd(sa)), rb = __builtin_amdgcn_rcpf(swapadd(sb));
#pragma unroll
  for (int i = 0; i < 5; ++i) { o[i] = pk2(a[2 * i] * ra, a[2 * i + 1] * ra); o[5 + i] = pk2(b[2 * i] * rb, b[2 * i + 1] * rb); }
}

#define STAMP(i)

#define ZERO16 f32x16{0.f, 0.f, 0.f, 0.f, 0.f, 0.f, 0.f, 0.f, 0.f, 0.f, 0.f, 0.f, 0.f, 0.f, 0.f, 0.f}
#define ZERO4 f32x4{0.f, 0.f, 0.f, 0.f}

DI void epi_kv(const f32x16& a0, const f32x16& a1, LAS char* L, int w, int lane, f32x16& kv0, f32x16& kv1) {
  const int hh = lane >> 5, tok = lane & 31;
  LAS f32x4* bt = (LAS f32x4*)(L + L_BIAST + w * 256 + hh * 128);
  float bs[24];
#pragma unroll
  for (int i = 0; i < 6; ++i) { const f32x4 t = bt[i]; bs[4 * i] = t.x; bs[4 * i + 1] = t.y; bs[4 * i + 2] = t.z; bs[4 * i + 3] = t.w; }
  float p[10]; unsigned o[10];
#pragma unroll
  for (int i = 0; i < 10; ++i) p[i] = fmaf(a0[i], LOG2E, bs[i]);
  softpair(p, o);
  LAS char* tb = L + L_TB + w * TB_SZ;
  LAS char* row = tb + tok * TB_ROW;
#pragma unroll
  for (int g = 0; g < 5; ++g) *(LAS v2u*)(row + 16 * g + 8 * hh) = v2u{o[2 * g], o[2 * g + 1]};
  float v[12];
#pragma unroll
  for (int i = 0; i < 6; ++i) { v[i] = a0[10 + i] + bs[10 + i]; v[6 + i] = a1[i] + bs[16 + i]; }
#pragma unroll
  for (int u = 0; u < 3; ++u) *(LAS v2u*)(row + 80 + 24 * hh + 8 * u) = v2u{pk2(v[4 * u], v[4 * u + 1]), pk2(v[4 * u + 2], v[4 * u + 3])};
  CFENCE();
  LAS char* rd = tb + (8 * hh + ((lane >> 2) & 3)) * TB_ROW + (16 * ((lane >> 4) & 1) + 4 * (lane & 3)) * 2;
#pragma unroll
  for (int ks = 0; ks < 2; ++ks) {
    LAS char* q = rd + 16 * ks * TB_ROW;
    const v2u A0a = trread(q), A0b = trread(q + 4 * TB_ROW), A1a = trread(q + 64), A1b = trread(q + 4 * TB_ROW + 64);
    const v2u Ba = trread(q + 80), Bb = trread(q + 4 * TB_ROW + 80);
    const v4u B = {Ba.x, Ba.y, Bb.x, Bb.y};
    kv0 = mfma32(v4u{A0a.x, A0a.y, A0b.x, A0b.y}, B, kv0);
    kv1 = mfma32(v4u{A1a.x, A1a.y, A1b.x, A1b.y}, B, kv1);
  }
  CFENCE();
}
DI void epi_q(const f32x16& a1, LAS char* L, int w, int lane, unsigned (&o)[10]) {
  LAS f32x4* bt = (LAS f32x4*)(L + L_BIAST + w * 256 + (lane >> 5) * 128);
  float bs[12];
#pragma unroll
  for (int i = 0; i < 3; ++i) { const f32x4 t = bt[5 + i]; bs[4 * i] = t.x; bs[4 * i + 1] = t.y; bs[4 * i + 2] = t.z; bs[4 * i + 3] = t.w; }
  float p[10];
#pragma unroll
  for (int i = 0; i < 10; ++i) p[i] = fmaf(a1[6 + i], LOG2E, bs[2 + i]);
  softpair(p, o);
}

DI float geglu1(float ag, float au, float csg, float bg, float csu, float bu, float rs, float mr) {
  const float g = fmaf(ag, rs, fmaf(-mr, csg, bg)), u = fmaf(au, rs, fmaf(-mr, csu, bu));
  const float e = __builtin_amdgcn_exp2f(g * fmaf(g * g, -0.10294324f, -2.3022082f));
  return g * __builtin_amdgcn_rcpf(1.f + e) * u;
}

__global__ __launch_bounds__(256, 1) void k_main(const u16* x, char* ws, u16* out) {
  extern __shared__ __attribute__((aligned(16))) char smem[];
  LAS char* L = (LAS char*)smem;
  const int lbase = (int)(size_t)L;
  const rsrc_t rs = __builtin_amdgcn_make_buffer_rsrc((void*)ws, 0, WS_BYTES, 0x00020000);
  const int b = blockIdx.x, lane = threadIdx.x & 63, w = __builtin_amdgcn_readfirstlane(threadIdx.x >> 6);
  const int xcd = b & 7, ii = b >> 3, v16 = lane * 16, hh = lane >> 5;
  const int hB = 4 * xcd + (ii >> 3), pB = ii & 7, rotb = ii >> 3;
  const int tgC = 4 * xcd + (ii >> 3), cgC = ii & 7;
  const int tgD = 4 * (xcd >> 1) + (ii >> 3), fgD = 8 * (xcd & 1) + (ii & 7);
  STAMP(0);

  unsigned fq[2][10];
  {
    const int kb = (w + rotb) & 3;
    const int mso = lane < 40 ? MODV_OFF + 2560 + kb * 640 + lane * 16 : SAB_OFF + kb * 320 + (lane < 60 ? lane - 40 : 19) * 16;
    const v4u ms = ldp(rs, mso, 0);
    v4u wp[20], xa[40], xb[40];
#pragma unroll
    for (int i = 0; i < 20; ++i) wp[i] = ldp(rs, v16, WP_OFF + hB * 81920 + kb * 20480 + i * 1024);
    const int xo = XH_OFF + (8 * pB + 2 * w) * 40960;
#pragma unroll
    for (int i = 0; i < 40; ++i) xa[i] = ldp(rs, v16, xo + ((i / 10 + rotb) & 3) * 10240 + (i % 10) * 1024);
    SCHED();
    {
      LAS char* row = L + L_TB + w * TB_SZ + (lane & 31) * TB_ROW;
      *(LAS v2u*)(row + 128 + 8 * hh) = v2u{0x3F803F80u, 0x3F803F80u};
    }
    LAS char* mst = L + L_MODST + w * 1024;
    *(LAS v4u*)(mst + v16) = ms;
    CFENCE();
    float bias0 = 0.f, bias1 = 0.f;
#pragma unroll
    for (int kk = 0; kk < 10; ++kk) {
      const f32x4 s0 = *(LAS f32x4*)(mst + kk * 64 + hh * 32), s1 = *(LAS f32x4*)(mst + kk * 64 + hh * 32 + 16);
      const v4u sa = *(LAS v4u*)(mst + 640 + kk * 32 + hh * 16);
#pragma unroll
      for (int ft = 0; ft < 2; ++ft) {
        const v4u v = wp[2 * kk + ft];
        float bb = ft ? bias1 : bias0;
        bb = dot2(v.x, sa.x, bb); bb = dot2(v.y, sa.y, bb); bb = dot2(v.z, sa.z, bb); bb = dot2(v.w, sa.w, bb);
        if (ft) bias1 = bb; else bias0 = bb;
        const v4u o = {pk2(bflo(v.x) * s0.x, bfhi(v.x) * s0.y), pk2(bflo(v.y) * s0.z, bfhi(v.y) * s0.w),
                       pk2(bflo(v.z) * s1.x, bfhi(v.z) * s1.y), pk2(bflo(v.w) * s1.z, bfhi(v.w) * s1.w)};
        *(LAS v4u*)(L + (20 * w + 2 * kk + ft) * 1024 + v16) = o;
      }
    }
    {
      LAS float* bp = (LAS float*)(L + L_BIASP) + (2 * w + hh) * 64 + (lane & 31);
      bp[0] = bias0; bp[32] = bias1;
    }
    SCHED();
#pragma unroll
    for (int i = 0; i < 20; ++i) xb[i] = ldp(rs, v16, xo + 40960 + ((i / 10 + rotb) & 3) * 10240 + (i % 10) * 1024);
    SCHED();
    BAR();
    STAMP(1);
    {
      LAS float* bp = (LAS float*)(L + L_BIASP) + lane;
      float s = 0.f;
#pragma unroll
      for (int i = 0; i < 8; ++i) s += bp[64 * i];
      const int rho = lane & 31, sg = 16 * hh + (rho & 3) + 4 * (rho >> 3);
      if (sg < 10 || sg >= 22) s *= LOG2E;
      ((LAS float*)(L + L_BIAST))[w * 64 + ((rho >> 2) & 1) * 32 + sg] = s;
    }
    f32x16 a0 = ZERO16, a1 = ZERO16, c0 = ZERO16, c1 = ZERO16, kv0 = ZERO16, kv1 = ZERO16;
    LAS v4u* W = (LAS v4u*)(L + v16);
    {
      v4u wf[4][2];
#pragma unroll
      for (int i = 0; i < 3; ++i) { wf[i][0] = W[(2 * i) * 64]; wf[i][1] = W[(2 * i + 1) * 64]; }
      sfor<80>(SLAM(k) {
        constexpr int k = decltype(k_c)::value;
        if constexpr (k + 3 < 80) { constexpr int kn = (k + 3) % 40; wf[(k + 3) & 3][0] = W[(2 * kn) * 64]; wf[(k + 3) & 3][1] = W[(2 * kn + 1) * 64]; }
        if constexpr (k < 40) {
          if constexpr ((k & 1) == 0) { constexpr int i = 20 + (k >> 1); xb[i] = ldp(rs, v16, xo + 40960 + ((i / 10 + rotb) & 3) * 10240 + (i % 10) * 1024); }
          a0 = mfma32(wf[k & 3][0], xa[k], a0); a1 = mfma32(wf[k & 3][1], xa[k], a1);
          SCHED();
        } else {
          c0 = mfma32(wf[k & 3][0], xb[k - 40], c0); c1 = mfma32(wf[k & 3][1], xb[k - 40], c1);
          if constexpr (k == 44) epi_kv(a0, a1, L, w, lane, kv0, kv1);
          if constexpr (k == 62) epi_q(a1, L, w, lane, fq[0]);
        }
      });
    }
    STAMP(2);
    epi_kv(c0, c1, L, w, lane, kv0, kv1);
    {
      LAS f32x4* kr = (LAS f32x4*)(L + L_KVR + w * 5120 + v16);
#pragma unroll
      for (int g = 0; g < 4; ++g) kr[64 * g] = f32x4{kv0[4 * g], kv0[4 * g + 1], kv0[4 * g + 2], kv0[4 * g + 3]};
      kr[256] = f32x4{kv1[0], kv1[1], kv1[2], kv1[3]};
    }
    BAR();
    {
      LAS f32x4* kr = (LAS f32x4*)(L + L_KVR + v16);
      const f32x4 s = (kr[64 * w] + kr[320 + 64 * w]) + (kr[640 + 64 * w] + kr[960 + 64 * w]);
      const int ko = KVP_OFF + (hB * 8 + pB) * 5120;
      stc(rs, v16, ko + w * 1024, __builtin_bit_cast(v4u, s));
      if (w == 1) {
        const f32x4 s4 = (kr[256] + kr[320 + 256]) + (kr[640 + 256] + kr[960 + 256]);
        stc(rs, v16, ko + 4096, __builtin_bit_cast(v4u, s4));
      }
    }
    DRAIN();
    BAR();
    if (w == 0 && lane < 8) signal(ws, C_KV, hB * 8 + lane);
    STAMP(3);
    if (w > 0) {
      dmaN<33>(rs, v16, WO_OFF + cgC * 102400 + (w - 1) * 33 * 1024, lbase + (w - 1) * 33 * 1024);
      if (w == 1) dma1(rs, v16, WO_OFF + cgC * 102400 + 99 * 1024, lbase + 99 * 1024);
      dmaN<12>(rs, v16, WGU_OFF + fgD * 286720 + (w - 1) * 12 * 1024, lbase + 3 * SLOT + (w - 1) * 12 * 1024);
    }
    epi_q(c1, L, w, lane, fq[1]);
  }
  {
    const int ks8 = tgD & 7, ft = (tgD >> 3) * 4 + w;
    if (ft < 7) {
      v4u wf[5], bf[5];
#pragma unroll
      for (int s = 0; s < 5; ++s) {
        const int k16 = 5 * ks8 + s;
        wf[s] = ldp(rs, v16, WGU_OFF + fgD * 286720 + (ft < 3 ? k16 * 3 + ft : 120 + (ft - 3) * 40 + k16) * 1024);
        bf[s] = ldp(rs, CBP_OFF + (lane & 1) * 1280 + hh * 16, k16 * 32);
      }
      f32x16 a = ZERO16;
#pragma unroll
      for (int s = 0; s < 5; ++s) a = mfma32(wf[s], bf[s], a);
      if ((lane & 31) < 2) {
#pragma unroll
        for (int g = 0; g < 4; ++g)
          stc(rs, (hh * 2 + (lane & 1)) * 64 + g * 16, CSB_OFF + ((fgD * 8 + ks8) * 7 + ft) * 256,
              __builtin_bit_cast(v4u, f32x4{a[4 * g], a[4 * g + 1], a[4 * g + 2], a[4 * g + 3]}));
      }
    }
  }
  if (w == 0) poll_ge(ws, C_KV, hB * 8 + pB, 8u);
  BAR();
  STAMP(4);
  {
    const int ko = KVP_OFF + hB * 8 * 5120;
    v4u t[8];
#pragma unroll
    for (int p = 0; p < 8; ++p) t[p] = ldc(rs, v16, ko + p * 5120 + w * 1024);
    f32x4 s = ZERO4;
#pragma unroll
    for (int p = 0; p < 8; ++p) s += __builtin_bit_cast(f32x4, t[p]);
    *(LAS v2u*)(L + L_KVA + (w >> 1) * 1024 + v16 + (w & 1) * 8) = v2u{pk2(s.x, s.y), pk2(s.z, s.w)};
    if (w == 0) {
#pragma unroll
      for (int p = 0; p < 8; ++p) t[p] = ldc(rs, v16, ko + p * 5120 + 4096);
      f32x4 s4 = ZERO4;
#pragma unroll
      for (int p = 0; p < 8; ++p) s4 += __builtin_bit_cast(f32x4, t[p]);
      *(LAS v4u*)(L + L_KVA + 2048 + v16) = v4u{pk2(s4.x, s4.y), pk2(s4.z, s4.w), 0u, 0u};
    }
    BAR();
    const v4u A0 = *(LAS v4u*)(L + L_KVA + v16), A1 = *(LAS v4u*)(L + L_KVA + 1024 + v16), A2 = *(LAS v4u*)(L + L_KVA + 2048 + v16);
#pragma unroll
    for (int T = 0; T < 2; ++T) {
      f32x16 o = ZERO16;
      o = mfma32(A0, v4u{fq[T][0], fq[T][1], fq[T][2], fq[T][3]}, o);
      o = mfma32(A1, v4u{fq[T][4], fq[T][5], fq[T][6], fq[T][7]}, o);
      o = mfma32(A2, v4u{fq[T][8], fq[T][9], 0u, 0u}, o);
      const float r = __builtin_amdgcn_rcpf(o[12] + 1e-6f);
      const int t16 = 16 * pB + 4 * w + 2 * T;
      const int vo = ((lane >> 4) & 1) * 20480 + (lane & 15) * 16;
      stc(rs, vo + hh * 256, OT_OFF + t16 * 20480 + (hB >> 1) * 1024 + (hB & 1) * 512,
          v4u{pk2(o[0] * r, o[1] * r), pk2(o[2] * r, o[3] * r), pk2(o[4] * r, o[5] * r), pk2(o[6] * r, o[7] * r)});
      if (hh == 0)
        stc2(rs, vo, OT_OFF + t16 * 20480 + (16 + (hB >> 3)) * 1024 + ((hB >> 1) & 3) * 256 + (hB & 1) * 8,
             v2u{pk2(o[8] * r, o[9] * r), pk2(o[10] * r, o[11] * r)});
    }
  }
  DRAIN();
  BAR();
  if (w == 0) {
    if (lane < 32) signal(ws, C_O, pB * 32 + lane);
    if (lane < 16) signal(ws, C_CSB, lane * 16 + fgD);
  }
  STAMP(5);

  unsigned x1p[10];
  const int cC = lane & 15, gC = lane >> 4;
  const int tokC = 64 * tgC + 16 * w + cC, colC = 80 * cgC + 20 * gC;
  {
    const u16* xr = x + (size_t)tokC * 640 + colC;
    const v4u xr0 = *(const v4u*)xr, xr1 = *(const v4u*)(xr + 8);
    const v2u xr2 = *(const v2u*)(xr + 16);
    v4u ga[5], cb[5];
#pragma unroll
    for (int i = 0; i < 5; ++i) { ga[i] = ldp(rs, colC * 4 + i * 16, MODV_OFF + 2 * 2560); cb[i] = ldp(rs, colC * 4 + i * 16, MODV_OFF + 4 * 2560); }
    if (w == 0) poll_ge(ws, C_O, tgC * 8 + cgC, 32u);
    BAR();
    STAMP(6);
    v4u of[20];
#pragma unroll
    for (int k = 0; k < 20; ++k) of[k] = ldc(rs, v16, OT_OFF + (4 * tgC + w) * 20480 + k * 1024);
    SCHED();
    f32x4 acc[5] = {ZERO4, ZERO4, ZERO4, ZERO4, ZERO4};
    LAS v4u* W = (LAS v4u*)(L + v16);
    v4u wf[2][5];
#pragma unroll
    for (int ct = 0; ct < 5; ++ct) wf[0][ct] = W[ct * 64];
#pragma unroll
    for (int k = 0; k < 20; ++k) {
      if (k + 1 < 20) {
#pragma unroll
        for (int ct = 0; ct < 5; ++ct) wf[(k + 1) & 1][ct] = W[((k + 1) * 5 + ct) * 64];
      }
#pragma unroll
      for (int ct = 0; ct < 5; ++ct) acc[ct] = mfma16(wf[k & 1][ct], of[k], acc[ct]);
      SCHED();
    }
    STAMP(7);
    const unsigned xin[10] = {xr0.x, xr0.y, xr0.z, xr0.w, xr1.x, xr1.y, xr1.z, xr1.w, xr2.x, xr2.y};
    float x1[20]; unsigned xs[10];
    float sum = 0.f;
#pragma unroll
    for (int ct = 0; ct < 5; ++ct) {
      const f32x4 g4 = __builtin_bit_cast(f32x4, ga[ct]), c4 = __builtin_bit_cast(f32x4, cb[ct]);
      const unsigned at0 = pk2(acc[ct].x, acc[ct].y), at1 = pk2(acc[ct].z, acc[ct].w);
      const unsigned m0 = pk2(g4.x * bflo(at0), g4.y * bfhi(at0)), m1 = pk2(g4.z * bflo(at1), g4.w * bfhi(at1));
      x1p[2 * ct] = pk2(bflo(xin[2 * ct]) + bflo(m0), bfhi(xin[2 * ct]) + bfhi(m0));
      x1p[2 * ct + 1] = pk2(bflo(xin[2 * ct + 1]) + bflo(m1), bfhi(xin[2 * ct + 1]) + bfhi(m1));
      x1[4 * ct] = bflo(x1p[2 * ct]); x1[4 * ct + 1] = bfhi(x1p[2 * ct]); x1[4 * ct + 2] = bflo(x1p[2 * ct + 1]); x1[4 * ct + 3] = bfhi(x1p[2 * ct + 1]);
      xs[2 * ct] = pk2(x1[4 * ct] * c4.x, x1[4 * ct + 1] * c4.y);
      xs[2 * ct + 1] = pk2(x1[4 * ct + 2] * c4.z, x1[4 * ct + 3] * c4.w);
      sum += (x1[4 * ct] + x1[4 * ct + 1]) + (x1[4 * ct + 2] + x1[4 * ct + 3]);
    }
    const int so = XS_OFF + (2 * tgC + (w >> 1)) * 40960 + 5 * cgC * 1024 + (w & 1) * 256;
    stc(rs, gC * 1024 + cC * 16, so, v4u{xs[0], xs[1], xs[2], xs[3]});
    stc(rs, gC * 1024 + 512 + cC * 16, so, v4u{xs[4], xs[5], xs[6], xs[7]});
    stc2(rs, 4096 + (gC >> 1) * 512 + cC * 16 + (gC & 1) * 8, so, v2u{xs[8], xs[9]});
    sum = swapadd(swap16add(sum));
    const float ml = sum * (1.f / 80.f);
    float m2 = 0.f;
#pragma unroll
    for (int i = 0; i < 20; ++i) { const float d = x1[i] - ml; m2 = fmaf(d, d, m2); }
    m2 = swapadd(swap16add(m2));
    if (gC == 0) stc2(rs, tokC * 64 + cgC * 8, ST_OFF, v2u{__builtin_bit_cast(unsigned, sum), __builtin_bit_cast(unsigned, m2)});
  }
  DRAIN();
  BAR();
  if (w == 0 && lane < 16) signal(ws, C_X, (tgC >> 1) * 16 + lane);
  STAMP(8);

  const int guo = WGU_OFF + fgD * 286720;
  const int wdo1 = WD_OFF + cgC * 276480 + w * 40960;
  const int wdo2 = WD_OFF + cgC * 276480 + (32 + (11 * w + 1) / 2) * 5120;
  v4u wdp[34];
  {
    if (w > 0) dmaN<35>(rs, v16, guo + w * 35 * 1024, lbase + (w - 1) * SLOT);
    if (w == 1) {
      poll_ge(ws, C_CSB, tgD * 16 + fgD, 16u);
#pragma unroll
      for (int rep = 0; rep < 2; ++rep) {
        const int i4 = lane + 64 * rep;
        if (i4 < 112) {
          f32x4 s = ZERO4;
#pragma unroll
          for (int k8 = 0; k8 < 8; ++k8) s += __builtin_bit_cast(f32x4, ldc(rs, i4 * 16, CSB_OFF + (fgD * 8 + k8) * 1792));
          *(LAS f32x4*)(L + L_CSB + i4 * 16) = s;
        }
      }
    }
    if (w == 0) poll_ge(ws, C_X, tgD * 16 + fgD, 16u);
    DRAIN();
    BAR();
    STAMP(9);
    const int t32 = 4 * tgD + w;
    v4u xf[40];
    const v4u st0 = ldc(rs, (lane & 31) * 64 + hh * 32, ST_OFF + t32 * 2048), st1 = ldc(rs, (lane & 31) * 64 + hh * 32 + 16, ST_OFF + t32 * 2048);
#pragma unroll
    for (int k = 0; k < 40; ++k) xf[k] = ldc(rs, v16, XS_OFF + t32 * 40960 + k * 1024);
    SCHED();
    float rsd, mr;
    f32x16 acc[7];
#pragma unroll
    for (int i = 0; i < 7; ++i) acc[i] = ZERO16;
    constexpr int PD = 4;
    v4u wf[PD + 1];
    auto wpos = [](int i) { return ((i / 35 + 3) & 3) * SLOT + (i % 35) * 1024; };
#pragma unroll
    for (int i = 0; i < PD; ++i) wf[i] = *(LAS v4u*)(L + wpos(i) + v16);
    const int ao = ACT_OFF + 2 * t32 * 55296;
    const int avo = ((lane >> 4) & 1) * 55296 + (lane & 15) * 16;
    float av[8];
    LAS f32x4* cs = (LAS f32x4*)(L + L_CSB + hh * 128);
    sfor<280>(SLAM(i) {
      constexpr int i = decltype(i_c)::value;
      if (i % 35 == 0 && i > 0) {
        const int s = i / 35 - 1;
        if (s >= 2 && s <= 5) wait_vm<9>();
        BAR_RAW();
        const int sl = lbase + ((s + 3) & 3) * SLOT + w * 9 * 1024;
        if (s < 4) dmaN<9>(rs, v16, guo + ((s + 4) * 35 + w * 9) * 1024, sl);
        else dmaN<9>(rs, v16, wdo1 + (s - 4) * 9 * 1024, sl);
      }
      if (i + PD < 280) wf[(i + PD) % (PD + 1)] = *(LAS v4u*)(L + wpos(i + PD) + v16);
      const int ft = i < 120 ? i % 3 : 3 + (i - 120) / 40, k = i < 120 ? i / 3 : (i - 120) % 40;
      acc[ft] = mfma32(wf[i % (PD + 1)], xf[k], acc[ft]);
      if (i == 100) {
        const f32x4 a = __builtin_bit_cast(f32x4, st0), c = __builtin_bit_cast(f32x4, st1);
        const float mu = swapadd((a.x + a.z) + (c.x + c.z)) * (1.f / 640.f);
        const float d0 = a.x * (1.f / 80.f) - mu, d1 = a.z * (1.f / 80.f) - mu, d2 = c.x * (1.f / 80.f) - mu, d3 = c.z * (1.f / 80.f) - mu;
        const float m2 = swapadd(((a.y + a.w) + (c.y + c.w)) + 80.f * ((d0 * d0 + d1 * d1) + (d2 * d2 + d3 * d3)));
        rsd = 1.f / rbf(sqrtf(rbf(rbf(m2 * (1.f / 640.f)) + 1e-5f)));
        mr = rbf(mu) * rsd;
      }
      if (i >= 120) {
        const int seg = (i - 120) / 40, kk = (i - 120) % 40;
        int et = -1, eh = 0;
        if (seg < 2) { if (kk % 10 == 2) { et = 2 * seg + kk / 20; eh = (kk / 10) & 1; } }
        else if (kk % 20 == 2) { et = 2 + seg; eh = kk / 20; }
        if (et >= 0) {
          const f32x4 cg_ = cs[et * 16 + eh], cu_ = cs[et * 16 + 2 + eh], bg_ = cs[et * 16 + 4 + eh], bu_ = cs[et * 16 + 6 + eh];
#pragma unroll
          for (int e = 0; e < 4; ++e) av[4 * eh + e] = geglu1(acc[et][4 * eh + e], acc[et][8 + 4 * eh + e], cg_[e], bg_[e], cu_[e], bu_[e], rsd, mr);
          if (eh == 1) {
            const int id = et < 4 ? 8 * fgD + 2 * et : 128 + 11 * (fgD >> 1) + 5 * (fgD & 1) + 2 * et - 8;
            stc(rs, avo + hh * 256, ao + id * 256, v4u{pk2(av[0], av[1]), pk2(av[2], av[3]), pk2(av[4], av[5]), pk2(av[6], av[7])});
          }
        }
      }
      if (i >= 176 && i < 244 && (i & 1) == 0) {
        const int n = 27 + (i - 176) / 2;
        wdp[n - 27] = ldp(rs, v16, n < 40 ? wdo1 + n * 1024 : wdo2 + (n - 40) * 1024);
      }
      if (i < 120 || i % 5 == 0 || i % 35 == 34) SCHED();
    });
    STAMP(10);
    {
#pragma unroll
      for (int eh = 0; eh < 2; ++eh) {
        const f32x4 cg_ = cs[96 + eh], cu_ = cs[96 + 2 + eh], bg_ = cs[96 + 4 + eh], bu_ = cs[96 + 6 + eh];
#pragma unroll
        for (int e = 0; e < 4; ++e) av[4 * eh + e] = geglu1(acc[6][4 * eh + e], acc[6][8 + 4 * eh + e], cg_[e], bg_[e], cu_[e], bu_[e], rsd, mr);
      }
      const int idb = 128 + 11 * (fgD >> 1);
      if (hh == 0) stc(rs, avo, ao + (idb + 5 * (fgD & 1) + 4) * 256, v4u{pk2(av[0], av[1]), pk2(av[2], av[3]), pk2(av[4], av[5]), pk2(av[6], av[7])});
      else stc2(rs, avo, ao + (idb + 10) * 256 + (fgD & 1) * 8, v2u{pk2(av[0], av[1]), pk2(av[2], av[3])});
    }
  }
  DRAIN();
  BAR();
  if (w == 0 && lane < 16) signal(ws, C_A2, tgD * 16 + lane);
  dmaN<9>(rs, v16, wdo2 + 21 * 1024, lbase + 2 * SLOT + w * 9 * 1024);
  STAMP(11);

  {
    v4u gb[5];
#pragma unroll
    for (int i = 0; i < 5; ++i) gb[i] = ldp(rs, colC * 4 + i * 16, MODV_OFF + 5 * 2560);
    if (w == 0) poll_ge(ws, C_A2, tgC * 8 + cgC, 16u);
    DRAIN();
    BAR();
    STAMP(12);
    const int ks2 = 32 + (11 * w + 1) / 2;
    v4u af[14][4];
    int tto[4];
#pragma unroll
    for (int u = 0; u < 4; ++u) tto[u] = ACT_OFF + (4 * tgC + ((w + u) & 3)) * 55296;
#pragma unroll
    for (int s = 0; s < 14; ++s)
#pragma unroll
      for (int u = 0; u < 4; ++u) {
        const int ks = s < 8 ? 8 * w + s : (s == 13 && (w & 1)) ? ks2 : ks2 + s - 8;
        af[s][u] = ldc(rs, v16, tto[u] + ks * 1024);
      }
    SCHED();
    f32x4 acc[4][5];
#pragma unroll
    for (int u = 0; u < 4; ++u)
#pragma unroll
      for (int ct = 0; ct < 5; ++ct) acc[u][ct] = ZERO4;
    LAS char* mine = L + w * 9 * 1024 + v16;
#pragma unroll
    for (int s = 0; s < 14; ++s) {
      if (s == 13 && (w & 1)) break;
#pragma unroll
      for (int ct = 0; ct < 5; ++ct) {
        const int n = 5 * s + ct;
        v4u wv;
        if (n < 27) wv = *(LAS v4u*)(mine + ((n / 9 + 3) & 3) * SLOT + (n % 9) * 1024);
        else if (n < 61) wv = wdp[n - 27];
        else wv = *(LAS v4u*)(mine + 2 * SLOT + (n - 61) * 1024);
#pragma unroll
        for (int u = 0; u < 4; ++u) acc[u][ct] = mfma16(wv, af[s][u], acc[u][ct]);
      }
    }
    STAMP(13);
    CFENCE();
#pragma unroll
    for (int u = 1; u < 4; ++u)
#pragma unroll
      for (int ct = 0; ct < 5; ++ct) { const int f = (u - 1) * 5 + ct; *(LAS f32x4*)(mine + (f >> 3) * SLOT + (f & 7) * 1024) = acc[u][ct]; }
    BAR();
    unsigned o[10];
#pragma unroll
    for (int ct = 0; ct < 5; ++ct) {
      f32x4 s = acc[0][ct];
#pragma unroll
      for (int u = 1; u < 4; ++u) { const int f = (u - 1) * 5 + ct; s += *(LAS f32x4*)(L + ((w - u) & 3) * 9 * 1024 + v16 + (f >> 3) * SLOT + (f & 7) * 1024); }
      const f32x4 g4 = __builtin_bit_cast(f32x4, gb[ct]);
      const unsigned f0 = pk2(s.x, s.y), f1 = pk2(s.z, s.w);
      const unsigned m0 = pk2(g4.x * bflo(f0), g4.y * bfhi(f0)), m1 = pk2(g4.z * bflo(f1), g4.w * bfhi(f1));
      o[2 * ct] = pk2(bflo(x1p[2 * ct]) + bflo(m0), bfhi(x1p[2 * ct]) + bfhi(m0));
      o[2 * ct + 1] = pk2(bflo(x1p[2 * ct + 1]) + bflo(m1), bfhi(x1p[2 * ct + 1]) + bfhi(m1));
    }
    u16* orow = out + (size_t)tokC * 640 + colC;
    *(v4u*)orow = v4u{o[0], o[1], o[2], o[3]};
    *(v4u*)(orow + 8) = v4u{o[4], o[5], o[6], o[7]};
    *(v2u*)(orow + 16) = v2u{o[8], o[9]};
  }
  STAMP(14);
}

extern "C" void kernel_launch(void* const* d_in, const int* in_sizes, int n_in, void* d_out, int out_size, void* d_ws, size_t ws_size,
                              hipStream_t stream) {
  (void)in_sizes; (void)n_in; (void)out_size; (void)ws_size;
  (void)hipFuncSetAttribute((const void*)k_main, hipFuncAttributeMaxDynamicSharedMemorySize, MAIN_LDS);
  k_prep<<<K1_GRID, 256, 0, stream>>>((const u16*)d_in[0], (const u16*)d_in[1], (const u16*)d_in[2], (const u16*)d_in[3], (const u16*)d_in[5],
                                      (const u16*)d_in[6], (const u16*)d_in[7], (const u16*)d_in[8], (const u16*)d_in[9], (char*)d_ws);
  k_main<<<256, 256, MAIN_LDS, stream>>>((const u16*)d_in[0], (char*)d_ws, (u16*)d_out);
}
```

```cpp
#include <hip/hip_runtime.h>
#include <hip/hip_bf16.h>
#include <utility>

typedef unsigned short u16;
typedef unsigned v4u __attribute__((ext_vector_type(4)));
typedef unsigned v2u __attribute__((ext_vector_type(2)));
typedef float f32x2 __attribute__((ext_vector_type(2)));
typedef float f32x4 __attribute__((ext_vector_type(4)));
typedef float f32x16 __attribute__((ext_vector_type(16)));
typedef __bf16 bf16x8 __attribute__((ext_vector_type(8)));
typedef __bf16 bf16x2 __attribute__((ext_vector_type(2)));
typedef short s16x4 __attribute__((ext_vector_type(4)));
#define LAS __attribute__((address_space(3)))
#define GAS __attribute__((address_space(1)))
#define DI __device__ __forceinline__
#define RLX_AGENT __ATOMIC_RELAXED, __HIP_MEMORY_SCOPE_AGENT

constexpr int S_ = 2048, D_ = 640, F_ = 1728;

constexpr int C_KV = 0, C_GU = 1, C_WD = 2, C_O = 3, C_X = 4, C_A1 = 5, C_A2 = 6, C_A3 = 7, NCNT = 8;
constexpr int CNT_OFF = 0;
constexpr int TMO_OFF = 0x80000 + 69632;
constexpr int MODP_OFF = 0x80000;
constexpr int XH_OFF = MODP_OFF + 73728;
constexpr int WP_OFF = XH_OFF + 64 * 40 * 1024;
constexpr int WO_OFF = WP_OFF + 32 * 80 * 1024;
constexpr int WGU_OFF = WO_OFF + 8 * 100 * 1024;
constexpr int WD_OFF = WGU_OFF + 16 * 280 * 1024;
constexpr int KVP_OFF = WD_OFF + 8 * 280 * 1024;
constexpr int CSB_OFF = KVP_OFF + 32 * 8 * 5 * 1024;
constexpr int OT_OFF = CSB_OFF + 16 * 8 * 7 * 256;
constexpr int XS_OFF = OT_OFF + 128 * 20 * 1024;
constexpr int ST_OFF = XS_OFF + 64 * 40 * 1024;
constexpr int ACT_OFF = ST_OFF + 8 * 2048 * 8;
constexpr int DESC_OFF = ACT_OFF + 128 * 56 * 1024;
constexpr int WS_BYTES = DESC_OFF + 1024 * 128;

constexpr float LOG2E = 1.4426950408889634f;
DI float bflo(unsigned p) { return __builtin_bit_cast(float, p << 16); }
DI float bfhi(unsigned p) { return __builtin_bit_cast(float, p & 0xffff0000u); }
DI unsigned pk2(float a, float b) { f32x2 v = {a, b}; return __builtin_bit_cast(unsigned, __builtin_convertvector(v, bf16x2)); }
DI float rbf(float a) { return bflo(pk2(a, a)); }
DI f32x16 mfma32(v4u a, v4u b, f32x16 c) { return __builtin_amdgcn_mfma_f32_32x32x16_bf16(__builtin_bit_cast(bf16x8, a), __builtin_bit_cast(bf16x8, b), c, 0, 0, 0); }
DI f32x4 mfma16(v4u a, v4u b, f32x4 c) { return __builtin_amdgcn_mfma_f32_16x16x32_bf16(__builtin_bit_cast(bf16x8, a), __builtin_bit_cast(bf16x8, b), c, 0, 0, 0); }
DI v2u trread(LAS char* p) { return __builtin_bit_cast(v2u, __builtin_amdgcn_ds_read_tr16_b64_v4i16((LAS s16x4*)p)); }
DI float dot2(unsigned a, unsigned b, float c) { return __builtin_amdgcn_fdot2_f32_bf16(__builtin_bit_cast(bf16x2, a), __builtin_bit_cast(bf16x2, b), c, false); }

DI int wo_row(int ks, int g, int j) {
  int head, d;
  if (ks < 16) { head = 2 * ks + (g >> 1); d = 8 * (g & 1) + j; }
  else { head = 8 * (ks - 16) + 2 * g + (j >> 2); d = 16 + (j & 3); }
  return 20 * head + d;
}
DI int act_row(int id, int j) {
  if (id < 96) { const int fg = id / 6; return 108 * fg + 8 * (id - 6 * fg) + j; }
  const int u = (id - 96) & 63, l = (id < 160 ? 48 : 80) + 8 * (u & 3) + j;
  return l < 108 ? 108 * (u >> 2) + l : -1;
}
DI int wd_pos(int ks, int cg) {
  if (ks < 24) { const int w = ks / 6, q = ks - 6 * w - (cg >= 6 ? cg - 6 : cg); return 14 * w + (q < 0 ? q + 6 : q); }
  const int u = (ks - 24) & 15;
  return 14 * (u >> 2) + (ks < 40 ? 6 : 10) + ((u - cg) & 3);
}

template <int... I, class F> DI void sfor_impl(std::integer_sequence<int, I...>, F&& f) { (f(std::integral_constant<int, I>{}), ...); }
template <int N, class F> DI void sfor(F&& f) { sfor_impl(std::make_integer_sequence<int, N>{}, f); }
#define SLAM(i) [&](auto i##_c) __attribute__((always_inline))
typedef __amdgpu_buffer_rsrc_t rsrc_t;
constexpr int OOB = 0x7f000000;
DI rsrc_t mkrs(const void* p, int bytes) { return __builtin_amdgcn_make_buffer_rsrc((void*)p, 0, bytes, 0x00020000); }
DI void dma_raw(rsrc_t rs, int voff, int soff, int lds) {
  asm volatile("s_mov_b32 m0, %0\n\ts_nop 0\n\tbuffer_load_dwordx4 %1, %2, %3 offen lds" ::"s"(lds), "v"(voff), "s"(rs), "s"(soff) : "memory");
}
#define ZERO16 f32x16{0.f, 0.f, 0.f, 0.f, 0.f, 0.f, 0.f, 0.f, 0.f, 0.f, 0.f, 0.f, 0.f, 0.f, 0.f, 0.f}
#define ZERO4 f32x4{0.f, 0.f, 0.f, 0.f}
#define DRAIN() asm volatile("s_waitcnt vmcnt(0)" ::: "memory")
#define BAR() asm volatile("s_waitcnt lgkmcnt(0)\n\ts_barrier" ::: "memory")
constexpr int J_FOLD = 0, N_FOLD = 160;
constexpr int J_XH = J_FOLD + N_FOLD, N_XH = 128;
constexpr int J_WO = J_XH + N_XH, N_WO = 80;
constexpr int J_CH = J_WO + N_WO, N_CH = 16;
constexpr int J_MOD = J_CH + N_CH, N_MOD = 240;
constexpr int K1_GRID = J_MOD + N_MOD;
constexpr int K1_LDS = 30720;
static_assert(J_FOLD % 8 == 0 && J_WO % 8 == 0 && J_MOD % 8 == 0 && J_CH % 8 == 0, "units that share cache lines are dealt to one XCD label");

DI void st_wt(char* ws, int off, v4u v) {
  __builtin_amdgcn_raw_buffer_store_b128(v, __builtin_amdgcn_make_buffer_rsrc((void*)ws, 0, WS_BYTES, 0x00020000), off, 0, 16);
}
DI float ror_add(float v) {
  v += __builtin_bit_cast(float, __builtin_amdgcn_update_dpp(0, __builtin_bit_cast(int, v), 0x128, 0xf, 0xf, false));
  v += __builtin_bit_cast(float, __builtin_amdgcn_update_dpp(0, __builtin_bit_cast(int, v), 0x124, 0xf, 0xf, false));
  v += __builtin_bit_cast(float, __builtin_amdgcn_update_dpp(0, __builtin_bit_cast(int, v), 0x122, 0xf, 0xf, false));
  v += __builtin_bit_cast(float, __builtin_amdgcn_update_dpp(0, __builtin_bit_cast(int, v), 0x121, 0xf, 0xf, false));
  return v;
}

DI void job_mod(int j, const u16* c, const u16* wmod, char* ws, LAS char* L) {
  const int t = threadIdx.x, lane = t & 63, w = __builtin_amdgcn_readfirstlane(t >> 6), cb = j >> 2, kq = j & 3;
  const rsrc_t rw = mkrs(wmod, 640 * 3840 * 2);
  const int vo = (lane >> 3) * 7680 + (lane & 7) * 16, img = (int)(size_t)L + w * 5120;
#pragma unroll
  for (int i = 0; i < 5; ++i) dma_raw(rw, vo, (160 * kq + 40 * w + 8 * i) * 7680 + 128 * cb, img + i * 1024);
  const unsigned cu = *(const unsigned*)(c + 160 * kq + 2 * min(t, 79));
  auto silu = [](float x) { return x * __builtin_amdgcn_rcpf(1.f + __builtin_amdgcn_exp2f(-LOG2E * x)); };
  LAS char* sv = L + 20480;
  *(LAS unsigned*)(sv + t * 4) = pk2(silu(bflo(cu)), silu(bfhi(cu)));
  DRAIN();
  BAR();
  LAS char* rd = L + (w >> 1) * 10240 + (8 * (lane >> 5) + ((lane >> 2) & 3)) * 128 + (w & 1) * 64 + ((lane >> 4) & 1) * 32 + (lane & 3) * 8;
  f32x16 a = ZERO16;
#pragma unroll
  for (int i = 0; i < 5; ++i) {
    const v2u b0 = trread(rd + i * 2048), b1 = trread(rd + i * 2048 + 512);
    a = mfma32(*(LAS v4u*)(sv + (w >> 1) * 160 + i * 32 + (lane >> 5) * 16), v4u{b0.x, b0.y, b1.x, b1.y}, a);
  }
  LAS float* red = (LAS float*)(L + 21504);
  if (lane < 32) red[w * 32 + lane] = a[0];
  BAR();
  if (t < 64) *(float*)(ws + MODP_OFF + (64 * cb + t) * 16 + kq * 4) = red[t] + red[64 + t];
}

DI void job_xhat(int j, const u16* x, char* ws, LAS char* L) {
  const int t = threadIdx.x, row = t >> 4, i = t & 15;
  v4u v[5];
#pragma unroll
  for (int jj = 0; jj < 5; ++jj) v[jj] = *(const v4u*)(x + (size_t)(16 * j + row) * 640 + 8 * (i + 16 * jj));
  float s0 = 0.f, s1 = 0.f;
#pragma unroll
  for (int jj = 0; jj < 5; ++jj) { s0 = dot2(v[jj].x, 0x3F803F80u, s0); s1 = dot2(v[jj].y, 0x3F803F80u, s1); s0 = dot2(v[jj].z, 0x3F803F80u, s0); s1 = dot2(v[jj].w, 0x3F803F80u, s1); }
  const float mean = ror_add(s0 + s1) * (1.f / 640.f);
  float f[40], q0 = 0.f, q1 = 0.f;
#pragma unroll
  for (int jj = 0; jj < 5; ++jj)
#pragma unroll
    for (int e = 0; e < 4; ++e) {
      const float a = bflo(v[jj][e]) - mean, b = bfhi(v[jj][e]) - mean;
      f[8 * jj + 2 * e] = a; f[8 * jj + 2 * e + 1] = b; q0 = fmaf(a, a, q0); q1 = fmaf(b, b, q1);
    }
  const float rs = __builtin_amdgcn_rcpf(rbf(__builtin_amdgcn_sqrtf(rbf(rbf(ror_add(q0 + q1) * (1.f / 640.f)) + 1e-5f))));
  const float sh = (mean - rbf(mean)) * rs;
#pragma unroll
  for (int jj = 0; jj < 5; ++jj)
    *(LAS v4u*)(L + (i + 16 * jj) * 272 + row * 16) = v4u{pk2(fmaf(f[8 * jj], rs, sh), fmaf(f[8 * jj + 1], rs, sh)), pk2(fmaf(f[8 * jj + 2], rs, sh), fmaf(f[8 * jj + 3], rs, sh)),
                                                          pk2(fmaf(f[8 * jj + 4], rs, sh), fmaf(f[8 * jj + 5], rs, sh)), pk2(fmaf(f[8 * jj + 6], rs, sh), fmaf(f[8 * jj + 7], rs, sh))};
  __syncthreads();
  const int dst = XH_OFF + (j >> 1) * 40960 + (j & 1) * 256 + i * 16;
#pragma unroll
  for (int it = 0; it < 5; ++it) {
    const int qq = row + 16 * it;
    st_wt(ws, dst + qq * 512, *(LAS v4u*)(L + qq * 272 + i * 16));
  }
}

DI void job_fold(int j, const u16* wqkv, const u16* wphi, char* ws, LAS char* L) {
  const int t = threadIdx.x, lane = t & 63, w = __builtin_amdgcn_readfirstlane(t >> 6), hg = j & 7, kb = j >> 3;
  constexpr int PHI = 24 * 1088;
  const int lb = (int)(size_t)L;
  {
    const int r4 = lane >> 4, ch = (lane & 15) - r4;
    const int vo = (ch >= 0 && ch < 10) ? r4 * 3840 + ch * 16 : OOB;
    const rsrc_t rq = mkrs(wqkv, 640 * 1920 * 2);
#pragma unroll
    for (int i = 0; i < 6; ++i) {
      const int ty = i >> 1, g = 2 * w + (i & 1);
      dma_raw(rq, vo, (32 * kb + 4 * g) * 3840 + ty * 1280 + 160 * hg, lb + (8 * ty + g) * 1088);
    }
    dma_raw(mkrs(wphi, 12800 * 2), t < 200 ? t * 16 : OOB, 3200 * hg, lb + PHI + w * 1024);
  }
  const int rho = lane & 31, hh = lane >> 5, hf = (rho >> 2) & 1, r = (rho & 3) + 4 * (rho >> 3);
  const int krow = 16 * (r >> 3) + 8 * hf + (r & 7);
  LAS char* ab = L + (krow >> 2) * 1088 + (krow & 3) * 272 + 40 * w;
  LAS u16* ph0[2]; LAS u16* ph1[2];
  v4u V[2][2]; float sc[2];
#pragma unroll
  for (int ft = 0; ft < 2; ++ft) {
    const int sg = 16 * ft + r;
    const bool isp = ft == 0 ? sg < 10 : sg >= 22;
    const int f = 10 * hf + (ft == 0 ? sg : sg - 22);
    ph0[ft] = (LAS u16*)(L + PHI + (isp ? 800 * w + 320 * hh + 2 * f : 3200));
    ph1[ft] = (LAS u16*)(L + PHI + ((isp && !hh) ? 800 * w + 640 + 2 * f : 3200));
    sc[ft] = isp ? LOG2E : 1.f;
    const int rp = 12 * hf + sg - 10;
    const int dv = (isp || rp >= 20) ? 64 : (rp < 4 ? rp : rp < 8 ? rp + 4 : rp < 12 ? rp - 4 : rp);
#pragma unroll
    for (int s = 0; s < 2; ++s) {
      const int rel = dv - 16 * s - 8 * hh, q = rel >> 1;
      const unsigned one = (rel & 1) ? 0x3F800000u : 0x00003F80u;
      V[ft][s] = v4u{q == 0 ? one : 0u, q == 1 ? one : 0u, q == 2 ? one : 0u, q == 3 ? one : 0u};
      asm volatile("" : "+v"(V[ft][s]));
    }
  }
  DRAIN();
  BAR();
  v4u A[3][2];
#pragma unroll
  for (int ty = 0; ty < 3; ++ty) {
    LAS char* b = ab + ty * 8704;
    const v2u a0 = *(LAS v2u*)(b + 16 * hh), a1 = *(LAS v2u*)(b + 16 * hh + 8), a2 = *(LAS v2u*)(b + 32);
    A[ty][0] = v4u{a0.x, a0.y, a1.x, a1.y};
    A[ty][1] = v4u{a2.x, a2.y, a2.x, a2.y};
  }
  const int dst = WP_OFF + (4 * hg + w) * 81920 + (2 * kb) * 2048 + lane * 16;
#pragma unroll
  for (int ft = 0; ft < 2; ++ft) {
    unsigned e[12];
#pragma unroll
    for (int jj = 0; jj < 8; ++jj) e[jj] = ph0[ft][jj * 20];
#pragma unroll
    for (int jj = 0; jj < 4; ++jj) e[8 + jj] = ph1[ft][jj * 20];
    const v4u P0 = {e[0] | (e[1] << 16), e[2] | (e[3] << 16), e[4] | (e[5] << 16), e[6] | (e[7] << 16)};
    const v4u P1 = {e[8] | (e[9] << 16), e[10] | (e[11] << 16), 0u, 0u};
    const int ty = ft == 0 ? 1 : 0;
    f32x16 a = mfma32(A[2][0], V[ft][0], ZERO16);
    a = mfma32(A[2][1], V[ft][1], a); a = mfma32(A[ty][0], P0, a); a = mfma32(A[ty][1], P1, a);
    a *= sc[ft];
#pragma unroll
    for (int s = 0; s < 2; ++s)
      st_wt(ws, dst + s * 2048 + ft * 1024, v4u{pk2(a[8 * s], a[8 * s + 1]), pk2(a[8 * s + 2], a[8 * s + 3]), pk2(a[8 * s + 4], a[8 * s + 5]), pk2(a[8 * s + 6], a[8 * s + 7])});
  }
}

DI void job_wo(int j, const u16* wsrc, char* ws, LAS char* L) {
  const int t = threadIdx.x, lane = t & 63, w = t >> 6, cg = j & 7, ks0 = 2 * (j >> 3);
  v4u v[3];
#pragma unroll
  for (int it = 0; it < 3; ++it) {
    int idx = t + 256 * it; if (idx >= 640) idx = 639;
    const int slot = idx / 10, ch = idx - 10 * slot;
    const int row = wo_row(ks0 + (slot >> 5), (slot >> 3) & 3, slot & 7);
    v[it] = *(const v4u*)(wsrc + (size_t)row * 640 + 80 * cg + 8 * ch);
  }
#pragma unroll
  for (int it = 0; it < 3; ++it) {
    int idx = t + 256 * it; if (idx >= 640) idx = 639;
    const int slot = idx / 10, ch = idx - 10 * slot;
    *(LAS v4u*)(L + slot * 176 + ch * 16) = v[it];
  }
  __syncthreads();
  const int g = lane >> 4, q = (lane >> 2) & 3, p = lane & 3;
  LAS char* base = L + (8 * g + q) * 176 + 40 * p;
  const int pb = (j >> 3) < 8 ? ((j >> 3) - cg) & 7 : j >> 3;
  const int dst = WO_OFF + cg * 102400 + pb * 10240 + lane * 16;
  for (int fr = w; fr < 10; fr += 4) {
    const int ksl = fr >= 5, ct = fr - 5 * ksl;
    const v2u a = trread(base + ksl * 32 * 176 + ct * 8);
    const v2u b = trread(base + (ksl * 32 + 4) * 176 + ct * 8);
    st_wt(ws, dst + fr * 1024, v4u{a.x, a.y, b.x, b.y});
  }
}

__global__ __launch_bounds__(256) void k_prep(const u16* x, const u16* wqkv, const u16* wphi, const u16* wo, const u16* c, const u16* wmod, char* ws) {
  __shared__ __attribute__((aligned(16))) char smem[K1_LDS];
  LAS char* L = (LAS char*)smem;
  const int b = blockIdx.x;
  unsigned pf = 0;
  if (b >= J_CH && b < J_MOD) {
    unsigned long long base; unsigned len;
    asm volatile("s_getpc_b64 %0\n.Lpf_here:\n\ts_mov_b32 %1, .Lkm_end-.Lpf_here" : "=s"(base), "=s"(len));
    const unsigned off = (((unsigned)(b - J_CH) >> 3) * 256u + threadIdx.x) * 128u;
    if (off < len) pf = *(const GAS unsigned*)(base + off);
  }
  if (b < J_XH) job_fold((b & 7) * 20 + (b >> 3), wqkv, wphi, ws, L);
  else if (b < J_WO) job_xhat(b - J_XH, x, ws, L);
  else if (b < J_CH) job_wo((b & 7) * 10 + ((b - J_WO) >> 3), wo, ws, L);
  else if (b >= J_MOD) job_mod((b & 7) * 30 + ((b - J_MOD) >> 3), c, wmod, ws, L);
  else {
    const int e = b - J_CH;
    if (e < NCNT) *(unsigned*)(ws + CNT_OFF + (e * 256 + threadIdx.x) * 256) = 0u;
    else if (e == NCNT) { if (threadIdx.x == 0) *(unsigned*)(ws + TMO_OFF) = 0u; }
    else if (e < NCNT + 5) {
      const int g = (e - NCNT - 1) * 256 + threadIdx.x, bb = g >> 2, w = g & 3, xcd = bb & 7, ii = bb >> 3;
      const int tgC = 4 * xcd + (ii >> 3), cg = ii & 7, tgD = 4 * (xcd >> 1) + (ii >> 3), fg = 8 * (xcd & 1) + (ii & 7);
      const int ks8 = tgD >> 1, fth = tgD & 1, un = min(tgC, 27), ftj = 4 * fth + w, pb8 = (ks8 - fg) & 7;
      int d[24];
      d[0] = MODP_OFF + ((w == 0 ? 2 : w == 2 ? 5 : 4) * 640 + 80 * (w < 3 ? cg : ks8)) * 16;
      d[1] = d[0] + 1024;
      d[2] = MODP_OFF + (3 * 640 + 80 * ks8 + 48 * w - 16) * 16;
      d[3] = ((80 * ks8 + 40 * (w & 1)) * F_ + 108 * fg + 64 * fth) * 2;
#pragma unroll
      for (int i = 0; i < 4; ++i) d[4 + i] = max(act_row(8 * un + 2 * w + (i >> 1), (i & 1) * 4), 0) * 1280 + 160 * cg;
      d[8] = WGU_OFF + fg * 286720 + (ftj < 3 ? 15 * pb8 + ftj : 120 + (min(ftj, 6) - 3) * 40 + 5 * pb8) * 1024;
      d[9] = ftj < 3 ? 3072 : 1024;
#pragma unroll
      for (int j = 0; j < 3; ++j) {
        const int fr = 3 - w + 4 * j, f9 = min(fr, 9), ksl = f9 >= 5;
        d[10 + j] = WD_OFF + cg * 286720 + wd_pos(2 * un + ksl, cg) * 5120 + (f9 - 5 * ksl) * 1024;
        d[16 + j] = ksl * 8192 + (f9 - 5 * ksl) * 8;
      }
      d[13] = CSB_OFF + ((fg * 8 + ks8) * 7 + min(ftj, 6)) * 256;
      d[14] = fth ? 6 : 8; d[15] = ftj == 6 ? 8 : 0; d[19] = 0;
      d[20] = ftj < 7 ? 0 : OOB; d[21] = tgC < 28 ? 0 : OOB; d[22] = (tgC < 28 && w >= 2) ? 0 : OOB; d[23] = 0;
#pragma unroll
      for (int q = 0; q < 8; ++q) *(v4u*)(ws + DESC_OFF + g * 128 + q * 16) = q < 6 ? v4u{(unsigned)d[4 * q], (unsigned)d[4 * q + 1], (unsigned)d[4 * q + 2], (unsigned)d[4 * q + 3]} : v4u{0u, 0u, 0u, 0u};
    }
  }
  asm volatile("" ::"v"(pf));
}
constexpr int SLOT = 36 * 1024, MISC = 4 * SLOT;
constexpr int L_TB = 81920, TB_ROW = 200, TB_SZ = 32 * TB_ROW;
constexpr int L_MODST = L_TB;
constexpr int L_RGU = 3 * SLOT, L_RWD = L_RGU + 20480;
constexpr int L_BIASF = MISC, L_KVA = MISC + 2048, L_CSF = MISC + 5120, L_PX = 2 * SLOT, L_PAR = MISC + 14336;
constexpr int MAIN_LDS = 163840;
constexpr unsigned SPIN_CAP = 1u << 16;

typedef int i32x16 __attribute__((ext_vector_type(16)));
typedef int i32x8 __attribute__((ext_vector_type(8)));
#define CAS __attribute__((address_space(4)))
DI void stc(rsrc_t rs, int voff, int soff, v4u v) {
  asm volatile("buffer_store_dwordx4 %0, %1, %2, %3 offen sc1\n\ts_nop 1" ::"v"(v), "v"(voff), "s"(rs), "s"(soff) : "memory");
}
DI void stc2(rsrc_t rs, int voff, int soff, v2u v) { asm volatile("buffer_store_dwordx2 %0, %1, %2, %3 offen sc1" ::"v"(v), "v"(voff), "s"(rs), "s"(soff) : "memory"); }
#define BAR_RAW() asm volatile("s_barrier" ::: "memory")
#define SCHED() __builtin_amdgcn_sched_barrier(0)
#define CFENCE() asm volatile("" ::: "memory")
template <int N> DI void wait_vm() { asm volatile("s_waitcnt vmcnt(%0)" ::"i"(N) : "memory"); }

template <bool C, int O = 0> DI void ald_a(v4u& d, rsrc_t rs, int voff, int soff) {
  if constexpr (C) asm volatile("buffer_load_dwordx4 %0, %1, %2, %3 offen offset:%c4 sc1" : "=a"(d) : "v"(voff), "s"(rs), "s"(soff), "i"(O));
  else asm volatile("buffer_load_dwordx4 %0, %1, %2, %3 offen offset:%c4" : "=a"(d) : "v"(voff), "s"(rs), "s"(soff), "i"(O));
}
template <bool C, int O = 0> DI void ald_v(v4u& d, rsrc_t rs, int voff, int soff) {
  if constexpr (C) asm volatile("buffer_load_dwordx4 %0, %1, %2, %3 offen offset:%c4 sc1" : "=v"(d) : "v"(voff), "s"(rs), "s"(soff), "i"(O));
  else asm volatile("buffer_load_dwordx4 %0, %1, %2, %3 offen offset:%c4" : "=v"(d) : "v"(voff), "s"(rs), "s"(soff), "i"(O));
}
template <int N> DI void wait_a2(v4u& d, v4u& e) { static_assert(N >= 0 && N < 64, ""); asm volatile("s_waitcnt vmcnt(%2)" : "+a"(d), "+a"(e) : "i"(N)); }
template <int N> DI void wait_a4(v4u& a, v4u& b, v4u& c, v4u& d) { static_assert(N >= 0 && N < 64, ""); asm volatile("s_waitcnt vmcnt(%4)" : "+a"(a), "+a"(b), "+a"(c), "+a"(d) : "i"(N)); }
DI void tie_a(v4u& d) { asm volatile("" : "+a"(d)); }
DI void tie_v(v4u& d) { asm volatile("" : "+v"(d)); }
DI void need(const v4u& d) { asm volatile("" ::"v"(d)); }
template <int N> DI void wait_a(v4u& d) { static_assert(N >= 0 && N < 64, ""); asm volatile("s_waitcnt vmcnt(%1)" : "+a"(d) : "i"(N)); }
template <int N> DI void wait_v(v4u& d) { static_assert(N >= 0 && N < 64, ""); asm volatile("s_waitcnt vmcnt(%1)" : "+v"(d) : "i"(N)); }
template <bool Z> DI void mfa(f32x16& c, const v4u& a, const v4u& b) {
  if constexpr (Z) asm volatile("v_mfma_f32_32x32x16_bf16 %0, %1, %2, 0" : "=&v"(c) : "v"(a), "a"(b));
  else asm volatile("v_mfma_f32_32x32x16_bf16 %0, %1, %2, %0" : "+v"(c) : "v"(a), "a"(b));
}
DI void mfv(f32x16& c, const v4u& a, const v4u& b) { asm volatile("s_nop 1\n\tv_mfma_f32_32x32x16_bf16 %0, %1, %2, %0" : "+v"(c) : "v"(a), "v"(b)); }
DI void mfz(f32x16& c, const v4u& a, const v4u& b) { asm volatile("s_nop 1\n\tv_mfma_f32_32x32x16_bf16 %0, %1, %2, 0" : "=&v"(c) : "v"(a), "a"(b)); }
DI void mfzv(f32x16& c, const v4u& a, const v4u& b) { asm volatile("s_nop 1\n\tv_mfma_f32_32x32x16_bf16 %0, %1, %2, 0" : "=&v"(c) : "v"(a), "v"(b)); }
DI void mf_settle(f32x16& a, f32x16& b) { asm volatile("s_nop 15" : "+v"(a), "+v"(b)); }
DI void mf_settle1(f32x16& a) { asm volatile("s_nop 15" : "+v"(a)); }
DI float vmax3(float a, float b, float c) { float r; asm("v_max3_f32 %0, %1, %2, %3" : "=v"(r) : "v"(a), "v"(b), "v"(c)); return r; }
DI float vmin3(float a, float b, float c) { float r; asm("v_min3_f32 %0, %1, %2, %3" : "=v"(r) : "v"(a), "v"(b), "v"(c)); return r; }
extern "C" __device__ float vmul(float a, float b) __asm("llvm.amdgcn.fmul.legacy");
template <int O> DI void ald2_v(v2u& d, rsrc_t rs, int voff, int soff) { asm volatile("buffer_load_dwordx2 %0, %1, %2, %3 offen offset:%c4 sc1" : "=v"(d) : "v"(voff), "s"(rs), "s"(soff), "i"(O)); }
template <int N> DI void wait2_v(v2u& d, v2u& e) { asm volatile("s_waitcnt vmcnt(%2)" : "+v"(d), "+v"(e) : "i"(N)); }
DI void ald_u(unsigned& d, rsrc_t rs, int soff) { asm volatile("buffer_load_dword %0, off, %1, %2 sc1" : "=v"(d) : "s"(rs), "s"(soff)); }
template <int N> DI void wait_u(unsigned& d) { asm volatile("s_waitcnt vmcnt(%1)" : "+v"(d) : "i"(N)); }
DI int opaque(int v) { asm volatile("" : "+v"(v)); return v; }

template <bool C, int J> DI void dmap(rsrc_t rs, int v16, int soff, int lds) {
  const int so = soff + (J & ~3) * 1024;
  if constexpr ((J & 3) == 0) asm volatile("s_mov_b32 m0, %0\n\ts_nop 0" ::"s"(lds + J * 1024) : "memory");
  if constexpr (C) asm volatile("buffer_load_dwordx4 %0, %1, %2 offen offset:%c3 sc1 lds" ::"v"(v16), "s"(rs), "s"(so), "i"((J & 3) * 1024) : "memory");
  else asm volatile("buffer_load_dwordx4 %0, %1, %2 offen offset:%c3 lds" ::"v"(v16), "s"(rs), "s"(so), "i"((J & 3) * 1024) : "memory");
}
template <bool C, int N> DI void dmaN(rsrc_t rs, int v16, int soff, int lds) { sfor<N>(SLAM(j) { dmap<C, decltype(j_c)::value>(rs, v16, soff, lds); }); }

DI GAS unsigned* cnt_ptr(char* ws, int type, int id) { return (GAS unsigned*)(ws + CNT_OFF + (type * 256 + id) * 256); }
DI void poll_ge(char* ws, int type, int id, unsigned want) {
  GAS unsigned* p = cnt_ptr(ws, type, id);
  GAS unsigned* tmo = (GAS unsigned*)(ws + TMO_OFF);
  for (unsigned spins = 0;;) {
    if (__builtin_amdgcn_readfirstlane(__hip_atomic_load(p, RLX_AGENT)) >= want) break;
    ++spins;
    if ((spins & 255u) == 0u) {
      if (__builtin_amdgcn_readfirstlane(__hip_atomic_load(tmo, RLX_AGENT)) != 0u) break;
      if (spins > SPIN_CAP) { __hip_atomic_store(tmo, 1u, RLX_AGENT); break; }
    }
  }
  DRAIN();
  __builtin_amdgcn_fence(__ATOMIC_ACQUIRE, "wavefront");
}
DI int sig_off(bool on, int type, int id) { return opaque(on ? CNT_OFF + (type * 256 + id) * 256 : OOB); }
DI void signal(rsrc_t rs, int voff) { asm volatile("buffer_atomic_add %0, %1, %2, 0 offen" ::"v"(1u), "v"(voff), "s"(rs) : "memory"); }

DI float swapmax(float v) { auto r = __builtin_amdgcn_permlane32_swap(__float_as_uint(v), __float_as_uint(v), false, false); return vmax3(__uint_as_float(r[0]), __uint_as_float(r[1]), __uint_as_float(r[1])); }
DI float swapmin(float v) { auto r = __builtin_amdgcn_permlane32_swap(__float_as_uint(v), __float_as_uint(v), false, false); return vmin3(__uint_as_float(r[0]), __uint_as_float(r[1]), __uint_as_float(r[1])); }
DI float swapadd(float v) { auto r = __builtin_amdgcn_permlane32_swap(__builtin_bit_cast(unsigned, v), __builtin_bit_cast(unsigned, v), false, false); return __uint_as_float(r[0]) + __uint_as_float(r[1]); }
DI float swap16add(float v) { auto r = __builtin_amdgcn_permlane16_swap(__builtin_bit_cast(unsigned, v), __builtin_bit_cast(unsigned, v), false, false); return __uint_as_float(r[0]) + __uint_as_float(r[1]); }

struct Soft { float e[20], mx, mn, t0, t1, d, sa, sb; unsigned o[10]; };
constexpr int NSM = 33;
template <int S, int B> DI void soft_slice(Soft& s, const f32x16& p) {
  if constexpr (S == 0) { s.t0 = vmax3(p[B], p[B + 1], p[B + 2]); s.t1 = vmax3(p[B + 3], p[B + 4], p[B + 5]); s.mx = vmax3(p[B + 6], p[B + 7], p[B + 8]); s.d = vmin3(p[B], p[B + 1], p[B + 2]); }
  else if constexpr (S == 1) { s.t0 = vmax3(s.t0, s.t1, p[B + 9]); s.t1 = vmin3(p[B + 3], p[B + 4], p[B + 5]); s.mn = vmin3(p[B + 6], p[B + 7], p[B + 8]); s.mx = vmax3(s.t0, s.mx, s.mx); }
  else if constexpr (S == 2) { s.d = vmin3(s.d, s.t1, p[B + 9]); s.mx = swapmax(s.mx); }
  else if constexpr (S == 3) { s.mn = swapmin(vmin3(s.d, s.mn, s.mn)); s.d = p[B] - s.mx; }
  else if constexpr (S < 24) {
    constexpr int j = S - 4;
    s.e[j] = __builtin_amdgcn_exp2f(s.d);
    if constexpr (j < 9) s.d = p[B + j + 1] - s.mx; else if constexpr (j < 19) s.d = s.mn - p[B + j - 9];
    if constexpr (j == 2) s.sa = s.e[0] + s.e[1]; else if constexpr (j > 2 && j <= 10) s.sa += s.e[j - 1];
    if constexpr (j == 12) s.sb = s.e[10] + s.e[11]; else if constexpr (j > 12) s.sb += s.e[j - 1];
  } else if constexpr (S == 24) { s.sa = swapadd(s.sa); s.sb += s.e[19]; }
  else if constexpr (S == 25) { s.sa = __builtin_amdgcn_rcpf(s.sa); s.sb = swapadd(s.sb); }
  else if constexpr (S == 26) { s.sb = __builtin_amdgcn_rcpf(s.sb); s.e[0] = vmul(s.e[0], s.sa); s.e[1] = vmul(s.e[1], s.sa); }
  else if constexpr (S < 32) {
    constexpr int g = S - 27;
#pragma unroll
    for (int i = 4 * g + 2; i < 4 * g + 6; ++i) if (i < 20) s.e[i] = vmul(s.e[i], i < 10 ? s.sa : s.sb);
    if constexpr (g > 0) s.o[2 * g - 1] = pk2(s.e[4 * g - 2], s.e[4 * g - 1]);
    s.o[2 * g] = pk2(s.e[4 * g], s.e[4 * g + 1]);
  } else s.o[9] = pk2(s.e[18], s.e[19]);
}
struct EpiB { Soft s; v2u t[12]; };
constexpr int NKV = NSM + 11;
template <int S> DI void kv_slice(EpiB& e, const f32x16& a0, const f32x16& a1, LAS char* row, LAS char* vw, LAS char* rd, f32x16& kv0, f32x16& kv1) {
  if constexpr (S < 3) {
    constexpr int g = 10 + 4 * S;
    auto V = [&](int i) __attribute__((always_inline)) { return i < 16 ? a0[i & 15] : a1[i & 15]; };
    *(LAS v2u*)(vw + 8 * S) = v2u{pk2(V(g), V(g + 1)), pk2(V(g + 2), V(g + 3))};
  } else if constexpr (S < NSM + 3) {
    soft_slice<S - 3, 0>(e.s, a0);
    if constexpr (S >= 31) *(LAS v2u*)(row + 16 * (S - 31)) = v2u{e.s.o[2 * (S - 31)], e.s.o[2 * (S - 31) + 1]};
  } else if constexpr (S < NSM + 7) {
    constexpr int z = S - NSM - 3, o = 6 * (z >> 1) + (z & 1);
    CFENCE();
    LAS char* q = rd + (16 * (z >> 1) + 4 * (z & 1)) * TB_ROW;
    e.t[o] = trread(q); e.t[o + 2] = trread(q + 64); e.t[o + 4] = trread(q + 80);
    CFENCE();
  } else {
    constexpr int z = S - NSM - 7, o = 6 * (z >> 1);
    const v4u B = {e.t[o + 4].x, e.t[o + 4].y, e.t[o + 5].x, e.t[o + 5].y};
    if constexpr ((z & 1) == 0) { kv0 = mfma32(v4u{e.t[o].x, e.t[o].y, e.t[o + 1].x, e.t[o + 1].y}, B, kv0); asm volatile("" : "+a"(kv0)); }
    else { kv1 = mfma32(v4u{e.t[o + 2].x, e.t[o + 2].y, e.t[o + 3].x, e.t[o + 3].y}, B, kv1); asm volatile("" : "+a"(kv1)); }
  }
}
constexpr int soft_cost(int s) { return s < 2 ? 18 : s < 4 ? 24 : s < 24 ? 17 : s < 26 ? 24 : s == 26 ? 17 : s < 31 ? 26 : s == 31 ? 17 : 5; }

constexpr int cap63(int n) { return n > 63 ? 63 : n; }
constexpr int B_RAW = 52, B_R0 = 27 + B_RAW, B_T0 = 124;
constexpr int b_xidx(int k) { return k < 4 ? 10 + k : k < 10 ? 15 + k : k < 16 ? 20 + k : k < B_RAW ? 27 + k : 36 + k; }
constexpr int b_widx(int f) { return f < 5 ? 5 + f : f < 10 ? 9 + f : f < 15 ? 15 + f : 21 + f; }
constexpr int b_xof(int n) { for (int k = 0; k < 80; ++k) if (b_xidx(k) == n) return k; return -1; }
constexpr int b_wof(int n) { for (int f = 0; f < 20; ++f) if (b_widx(f) == n) return f; return -1; }
constexpr int B_PRE = 14, B_N = 116;
enum { OP_MAIN, OP_DSR, OP_XW, OP_BAR, OP_VM, OP_AND, OP_SMF, OP_CVT, OP_WR, OP_MISC, OP_KV, OP_Q, OP_TRD, OP_TST, OP_TMF, OP_BMF };
struct Op { short k, i, n; };
struct Item { short k, i, cost, at, due, lag; };
constexpr int B_P = 23;
struct BProg { Op op[900]; int n, qdone; bool ok; };
constexpr BProg b_make() {
  BProg P{};
  P.ok = true;
  Item S[6][64] = {};
  int len[6] = {}, pos[6] = {}, last[6] = {};
  auto add = [&](int s, int k, int i, int cost, int at, int due, int lag) { S[s][len[s]++] = Item{(short)k, (short)i, (short)cost, (short)at, (short)due, (short)lag}; };
  for (int f = 5; f < 20; ++f) {
    const int s = (f & 1) ^ 1, d = f < 10 ? 2 * (f - 5) : 4 * (f - 10) + 11;
    add(s, OP_AND, f, 22, d - 8, d, 0); add(s, OP_SMF, f, 0, 0, 0, 0); add(s, OP_CVT, f, 22, 0, d + 2, 3); add(s, OP_WR, f, 18, 0, d + 3, 0);
  }
  for (int j = 1; j < 10; ++j) add(2, OP_MISC, j, 20, 52, 58 + 2 * j, 1);
  for (int j = 0; j < NKV; ++j) add(3, OP_KV, j, j < 3 ? 14 : j < NSM + 3 ? soft_cost(j - 3) + (j >= 31 ? 5 : 0) : j < NSM + 7 ? 15 : 0, 82, 84 + (6 * j) / 4, j == NSM + 7 ? 2 : 1);
  for (int j = 0; j < 8; ++j) { add(4, OP_TRD, j, j < 5 ? 20 : 15, B_T0, B_T0 + 4 * j, 0); add(4, OP_TST, j, 26, 0, B_T0 + 4 * j + 2, 2); if (j < 5) add(4, OP_TMF, j, 0, 0, 0, 0); }
  add(4, OP_TST, 8, 26, 0, B_T0 + 33, 1);
  for (int j = 0; j < NSM; ++j) add(5, OP_Q, j, soft_cost(j), 0, 999, 1);
  int n = 0, vm = B_P, cnt = B_P, gap = 0, vidx[128] = {};
  for (int i = 0; i < B_P; ++i) vidx[i] = i;
  auto emit = [&](int k, int i, int w) { P.op[n++] = Op{(short)k, (short)i, (short)(w > 63 ? 63 : w)}; };
  for (int m = 0; m < 160; ++m) {
    const int k = m >> 1;
    if (m == 12 || m == 32 || m == 52) {
      const int need = m == 12 ? 10 : m == 32 ? 15 : 20;
      for (int s = 0; s < 2; ++s) if (pos[s] < len[s] && S[s][pos[s]].i < need) P.ok = false;
      if (m == 52) emit(OP_MISC, 0, 0);
      emit(OP_BAR, m, 0);
    }
    if (m == 79 && pos[2] < len[2]) P.ok = false;
    if (m == B_T0) { if (vm < B_R0 + 9) P.ok = false; emit(OP_BAR, m, cnt - 1 - vidx[B_R0 + 8]); }
    if (m % 8 == 0 || m < 8) { const int q = m < 8 ? k : k + 3; if (m >= 8 || (m & 1) == 0) { if (b_xidx(q) >= vm) P.ok = false; emit(OP_XW, m, cnt - 1 - vidx[b_xidx(q)]); } }
    emit(OP_MAIN, m, 0);
    if (m == 79 || m == 159) { emit(OP_BMF, m, 0); gap += 2; }
    int used = 0;
    if (m + 8 < 160) emit(OP_DSR, m + 8, 0);
    for (bool again = true; again;) {
      again = false;
      ++gap;
      auto ready = [&](int s) {
        if (pos[s] >= len[s] || (s == 5 && pos[3] < NSM + 3)) return false;
        const Item it = S[s][pos[s]];
        return m >= it.at && gap >= last[s] + it.lag && !(it.k == OP_AND && b_widx(it.i) + 4 >= vm);
      };
      auto put_vm = [&]() { const bool raw = vm >= B_R0 && vm < B_R0 + 9; emit(OP_VM, vm, 0); vidx[vm++] = cnt++; used += raw ? 26 : 14; };
      auto put = [&](int s) {
        const Item it = S[s][pos[s]];
        emit(it.k, it.i, it.k == OP_AND ? cnt - 1 - vidx[b_widx(it.i)] : 0);
        if (it.k == OP_TST) ++cnt;
        used += it.cost; last[s] = gap; ++pos[s];
      };
      const int hi = b_xidx(k + 22 > 79 ? 79 : k + 22) + 1, lo = b_xidx(k + 13 > 79 ? 79 : k + 13) + 1;
      bool due = false;
      for (int s = 0; s < 5; ++s) if (ready(s) && S[s][pos[s]].cost > 0 && m >= S[s][pos[s]].due) due = true;
      if (vm < B_N && (vm < lo || (vm < hi && !due && ((gap & 1) || vm + 8 < hi)))) put_vm();
      for (int s = 0; s < 5; ++s) while (ready(s) && S[s][pos[s]].cost > 0 && m >= S[s][pos[s]].due) put(s);
      for (int s = 0; s < 6; ++s) while (ready(s) && S[s][pos[s]].cost > 0 && (used == 0 || used + S[s][pos[s]].cost <= 22)) put(s);
      if (used == 0 && vm < hi) put_vm();
      for (int s = 0; s < 5; ++s) if (ready(s) && S[s][pos[s]].cost == 0) { put(s); again = true; used = 0; break; }
    }
  }
  for (int s = 0; s < 5; ++s) if (pos[s] < len[s]) P.ok = false;
  if (vm < B_N) P.ok = false;
  P.qdone = pos[5];
  P.n = n;
  return P;
}
constexpr BProg BP = b_make();
static_assert(BP.ok, "phase B program");

constexpr int WO_0 = 10, WO_1 = 14, WO_2 = 18;
constexpr int WL_D = 0;
constexpr int WP_2 = 10, WP_C = 5, WP_3 = 4;
constexpr int D_XP = 12, NES = 22;
constexpr int d_xstep(int k) { return k < 30 ? 2 * (k - D_XP) : 38 + 4 * (k - 30); }
constexpr int d_dstep(int g, int j) { return g == 0 ? (j < 7 ? 36 + 4 * j : 45 + 2 * j) : g == 5 ? 208 + 2 * j : g == 6 ? 226 + 2 * j : 36 * (g + 1) - 8 + 4 * j; }
constexpr int D_NA = 16, D_PL = 236, D_PC = 279 - 2 * D_NA, D_P2 = 256;
struct DSched { int x[281], d[281], p[281], a[281], e[281], cnt[281], st[6]; bool ok; };
constexpr DSched d_make() {
  DSched t{};
  t.ok = true;
  for (int i = 0; i <= 280; ++i) { t.x[i] = -1; t.d[i] = -1; t.p[i] = -1; t.a[i] = -1; t.e[i] = -1; }
  for (int k = D_XP; k < 40; ++k) t.x[d_xstep(k)] = k;
  for (int g = 0; g < 7; ++g) for (int j = 0; j < (g == 4 ? 7 : 9); ++j) { const int i = d_dstep(g, j); if (t.x[i] >= 0 || t.d[i] >= 0) t.ok = false; t.d[i] = g * 9 + j; }
  int n = WP_2 + WP_C + WP_3;
  for (int i = 78; i < 208 && n < 34 + WL_D; i += 2) if (t.d[i] < 0) t.p[i] = n++;
  if (n < 34 + WL_D) t.ok = false;
  for (int c = 0; c < D_NA; ++c) t.a[D_PC + 1 + 2 * c] = c;
  int i = 121;
  for (int tl = 0; tl < 6; ++tl) {
    if (tl >= 3 && i < 120 + 40 * (tl - 2) + 2) i = 120 + 40 * (tl - 2) + 2;
    for (int s = 0; s < NES; ++s) {
      while ((s == NES - 1 && (i & 1) == 0) || i == D_PC || (tl == 5 && s > 0 && s % 2 == 0 && (i & 1) == 0)) ++i;
      if (s == NES - 1) t.st[tl] = i;
      t.e[i++] = tl * NES + s;
    }
  }
  if (i > 280 || t.st[2] > 196 || t.st[4] > D_P2 - 14) t.ok = false;
  int c = D_XP + 4;
  for (int j = 0; j <= 280; ++j) { c += (t.x[j] >= 0) + (t.d[j] >= 0) + (t.p[j] >= 0) + (t.a[j] >= 0) + (j == D_PL + 1) + (t.e[j] >= 0 && t.e[j] % NES == NES - 1); t.cnt[j] = c; }
  return t;
}
constexpr DSched DS = d_make();
static_assert(DS.ok, "phase D schedule");
constexpr int d_xidx(int k) { return k < D_XP ? k : DS.cnt[d_xstep(k)] - 1; }

constexpr int E_P3 = 8, E_C2 = 6, E_L3 = 21, E_C3 = 21 + 6;
struct ESched { int op[80], n, cnt[57], aidx[56], widx[18], p2, p3; bool ok; };
constexpr ESched e_make() {
  ESched t{};
  t.ok = true;
  int n = 0, a = D_NA, wl = WL_D;
  auto push = [&](int o) { if (o < 100) t.aidx[o] = n; else if (o < 200) t.widx[o - 100] = n; else if (o == 200) t.p2 = n; else t.p3 = n; t.op[n++] = o; };
  push(200); for (int i = 0; i < 4; ++i) push(a++);
  t.cnt[0] = n;
  for (int g = 0; g < 56; ++g) {
    const int lim = g < E_C2 ? 24 : g < E_C3 ? 40 : 56;
    if (g == E_L3) push(201);
    int k = (g % 3 == 0) ? 2 : 1;
    if (g >= E_C3) k = 2;
    while (k--) { if (a < lim) push(a++); else if (wl < 18) push(100 + wl++); }
    t.cnt[g + 1] = n;
  }
  t.n = n;
  for (int g = D_NA; g < 56; ++g) if (t.aidx[g] >= t.cnt[g - 7]) t.ok = false;
  for (int j = WL_D; j < 18; ++j) if (t.widx[j] >= t.cnt[4 * ((52 + j) / 5) - 8]) t.ok = false;
  if (a < 56 || wl < 18) t.ok = false;
  return t;
}
constexpr ESched ES = e_make();
static_assert(ES.ok, "phase E schedule");

__global__ __launch_bounds__(256, 1) void k_main(const u16* x, const u16* wg, const u16* wu, const u16* wd, char* ws, u16* out) {
  extern __shared__ __attribute__((aligned(16))) char smem[];
  LAS char* L = (LAS char*)smem;
  const int lbase = (int)(size_t)L;
  const rsrc_t rs = __builtin_amdgcn_make_buffer_rsrc((void*)ws, 0, WS_BYTES, 0x00020000);
  const int b = blockIdx.x, tid = threadIdx.x, lane = tid & 63, w = __builtin_amdgcn_readfirstlane(tid >> 6);
  const int xcd = b & 7, ii = b >> 3, v16 = lane * 16, hh = lane >> 5;
  const int hB = 8 * (xcd & 3) + (ii & 7), pB = 4 * (xcd >> 2) + (ii >> 3);
  const int rotb = (ii + (ii >> 3)) & 3, flip = (ii >> 2) & 1;
  const int tgC = 4 * xcd + (ii >> 3), cgC = ii & 7;
  const int tgD = 4 * (xcd >> 1) + (ii >> 3), fgD = 8 * (xcd & 1) + (ii & 7);
  const int guo = WGU_OFF + fgD * 286720, woo = WO_OFF + cgC * 102400;
  const int cC = lane & 15, gC = lane >> 4;
  const int vv[3] = {v16, opaque(v16 + 4096), opaque(v16 + 8192)};
  asm volatile(".Lkm_begin:");

  unsigned fq[2][10];
  auto wo_run = [&](auto a_c, auto b_c) __attribute__((always_inline)) {
    constexpr int a = decltype(a_c)::value, b = decltype(b_c)::value, m = b < 16 ? b : 16, c = a > 16 ? a : 16;
    if constexpr (a < m) dmaN<false, m - a>(rs, v16, woo + (36 + 16 * w + a) * 1024, lbase + (16 * w + a) * 1024);
    if constexpr (c < b) dmaN<false, b - c>(rs, v16, woo + (9 * w + c - 16) * 1024, lbase + 3 * SLOT + (9 * w + c - 16) * 1024);
  };
  const int rvo = (lane & 15) < 10 ? (lane >> 4) * 1280 + (lane & 15) * 16 : OOB;
  int kvsrc, kvdst;
  {
    const int col = lane & 31, u = col < 24 ? col - 20 : col < 28 ? col - 21 : col - 22;
    const bool spare = col >= 20 && col != 24 && col != 28;
    const int e = 6 * w + u, c5 = e < 20 ? e : e == 20 ? 24 : 28;
    const bool use = spare && u < 6 && e < 22;
    kvsrc = opaque(use ? 4096 + (hh * 32 + c5) * 16 : w * 1024 + v16);
    kvdst = opaque(use ? 2048 + (hh * 32 + c5) * 16 : (w >> 1) * 1024 + v16 + (w & 1) * 8);
  }
  unsigned xr[10];
  {
    const i32x16 ds = *(const CAS i32x16*)(ws + DESC_OFF + (b * 4 + w) * 128);
    const i32x8 dt = *(const CAS i32x8*)(ws + DESC_OFF + (b * 4 + w) * 128 + 64);
    const int xo = XH_OFF + (8 * pB + 2 * w) * 40960, k5 = 5 * (w >> 1);
    int xblk[2][4], wq[4];
#pragma unroll
    for (int i = 0; i < 4; ++i) {
      const int sb = (i + rotb) & 3;
      xblk[0][i] = xo + flip * 40960 + sb * 10240; xblk[1][i] = xo + (flip ^ 1) * 40960 + sb * 10240;
      wq[i] = WP_OFF + hB * 81920 + ((10 * sb + k5) * 2 + (w & 1)) * 1024;
    }
    v4u ms[5], mv[2], wp[20], xa[80];
    const rsrc_t rgu = mkrs(w < 2 ? wg : wu, D_ * F_ * 2), rd = mkrs(wd, D_ * F_ * 2);
    int gvo, mvo;
    auto msum = [](const v4u& p) { const f32x4 f = __builtin_bit_cast(f32x4, p); return rbf((f.x + f.y) + (f.z + f.w)); };
    auto issue = [&](auto n_c) __attribute__((always_inline)) {
      constexpr int n = decltype(n_c)::value;
      if constexpr (n <= 4) ald_v<false>(ms[n], rs, v16, MODP_OFF + n * 4096 + w * 1024);
      else if constexpr (b_wof(n) >= 0) { constexpr int f = b_wof(n), j = f % 5; ald_a<false, (j & 1) * 2048>(wp[f], rs, vv[j >> 1], wq[f / 5]); }
      else if constexpr (b_xof(n) >= 0) { constexpr int i = b_xof(n), r = i % 10; ald_a<false, (r & 3) * 1024>(xa[i], rs, vv[r >> 2], xblk[i / 40][(i % 40) / 10]); }
      else if constexpr (n == 41) ald_v<false>(mv[0], rs, v16, ds[0]);
      else if constexpr (n == 42) ald_v<false>(mv[1], rs, mvo, 0);
      else if constexpr (n < B_R0 + 5) dma_raw(rgu, gvo + (n - B_R0) * 8 * F_ * 2, 0, lbase + L_RGU + (5 * w + n - B_R0) * 1024);
      else dma_raw(rd, rvo, ds[4 + n - B_R0 - 5], lbase + L_RWD + (4 * w + n - B_R0 - 5) * 1024);
    };
    sfor<5>(issue);
    SCHED();
    sfor<B_PRE - 5>(SLAM(j) { issue(std::integral_constant<int, 5 + decltype(j_c)::value>{}); });
    SCHED();
    const int sbi = 48 * w + lane - 16;
    const bool mvt = lane < 16, mv1 = mvt && (w & 1);
    const int mvf = opaque(lbase + L_PAR + (mvt ? w * 320 + 256 + lane * 4 : 1280));
    const int mvh = opaque(lbase + L_PAR + ((mvt ? w == 3 : sbi < 80) ? (mvt ? 1536 + 128 : 1536 + 128 + 96 * w) + lane * 2 : 1920));
    const float mva = mv1 ? 1.f : 0.f;
    v4u dmk;
    {
      const int rho = lane & 31, r = (rho & 3) + 4 * (rho >> 3);
      const unsigned one = rho >= 16 ? 0xffffffffu : ((rho >> 2) & 1) != hh ? 0u : (r & 1) ? 0xffff0000u : 0x0000ffffu;
      dmk = v4u{(rho >= 16 || (r >> 1) == 0) ? one : 0u, (rho >= 16 || (r >> 1) == 1) ? one : 0u, (rho >= 16 || (r >> 1) == 2) ? one : 0u, (rho >= 16 || (r >> 1) == 3) ? one : 0u};
    }
    tie_v(dmk);
    LAS char* msc[4];
#pragma unroll
    for (int i = 0; i < 4; ++i) msc[i] = L + L_MODST + ((lane & 16) ? 0 : 1280) + (10 * ((i + rotb) & 3) + k5) * 32 + hh * 16;
    LAS char* wdst[2] = {L + (2 * k5 + (w & 1)) * 1024 + v16, (LAS char*)(size_t)opaque(lbase + (40 + 2 * k5 + (w & 1)) * 1024 + v16)};
    {
      LAS u16* ta = (LAS u16*)(L + L_MODST) + tid;
      wait_v<B_PRE - 5>(ms[4]);
      tie_v(ms[0]); tie_v(ms[1]); tie_v(ms[2]); tie_v(ms[3]);
      ta[0] = (u16)(__float_as_uint(msum(ms[0])) >> 16);
      ta[256] = (u16)(__float_as_uint(msum(ms[1])) >> 16);
      ta[512] = (u16)pk2(msum(ms[2]) + (w < 2 ? 0.f : 1.f), 0.f);
      ta[768] = (u16)pk2(msum(ms[3]) + 1.f, 0.f);
      ta[1024] = (u16)pk2(msum(ms[4]) + 1.f, 0.f);
    }
    sfor<5>(SLAM(j) { issue(std::integral_constant<int, B_PRE + decltype(j_c)::value>{}); });
    BAR();
    float bias = 0.f;
    v4u sv[2];
    f32x16 sd[2];
    auto sc_rd = [&](int f) __attribute__((always_inline)) { sv[f & 1] = *(LAS v4u*)(msc[f / 5] + (f % 5) * 32); };
    {
      v4u s5[5];
      f32x16 d5[5];
#pragma unroll
      for (int f = 0; f < 5; ++f) s5[f] = *(LAS v4u*)(msc[0] + f * 32);
      sc_rd(5); sc_rd(6);
      sfor<5>(SLAM(f) { constexpr int f = decltype(f_c)::value; wait_a<B_PRE + 5 - 1 - b_widx(f)>(wp[f]); const v4u a = s5[f] & dmk; mfz(d5[f], a, wp[f]); });
      sfor<5>(SLAM(f) {
        constexpr int f = decltype(f_c)::value;
        if constexpr (f == 3) mf_settle(d5[3], d5[4]);
        asm volatile("" : "+v"(d5[f]));
        *(LAS v4u*)(wdst[0] + 2 * f * 1024) = v4u{pk2(d5[f][0], d5[f][1]), pk2(d5[f][2], d5[f][3]), pk2(d5[f][4], d5[f][5]), pk2(d5[f][6], d5[f][7])};
        bias += d5[f][8];
      });
      asm volatile("" : "+v"(bias));
    }
    const int gam = lane >> 4, tq = (lane >> 2) & 3, tp = lane & 3;
    mvo = opaque((mvt || sbi < 80) ? (mvt ? ds[1] : ds[2]) + v16 : OOB);
    gvo = opaque((lane & 7) < ds[14] ? ds[3] + (lane >> 3) * F_ * 2 + (lane & 7) * 16 : OOB);
    BAR();
    sfor<B_P - B_PRE - 5>(SLAM(j) { issue(std::integral_constant<int, B_PRE + 5 + decltype(j_c)::value>{}); });
    f32x16 a0, a1, c0, c1, kv0 = ZERO16, kv1 = ZERO16;
    LAS char* W0 = L + v16;
    LAS char* W1 = (LAS char*)(size_t)opaque(lbase + v16 + 40960);
    EpiB e;
    LAS char* tb = L + L_TB + w * TB_SZ;
    LAS char *trow, *tvw, *trd, *gb0, *gb1, *dbase, *cp0;
    v4u bf0, bf1, ones, sa2[2], so2[2], tl;
    v2u tt[4];
    f32x16 csa = ZERO16;
    float pv;
    auto LP = [&](int v) __attribute__((always_inline)) { return (LAS char*)(size_t)opaque(v); };
    {
      v4u wf[12];
#pragma unroll
      for (int i = 0; i < 8; ++i) wf[i] = *(LAS v4u*)(W0 + i * 1024);
      sfor<BP.n>(SLAM(p) {
        constexpr Op o = BP.op[decltype(p_c)::value];
        constexpr int i = o.i;
        if constexpr (o.k == OP_MAIN) {
          constexpr int k = i >> 1;
          if constexpr (i % 4 == 0) need(wf[(i + 3) % 12]);
          mfa<k % 40 == 0>(k < 40 ? ((i & 1) ? a1 : a0) : ((i & 1) ? c1 : c0), wf[i % 12], xa[k]);
        } else if constexpr (o.k == OP_DSR) { constexpr int n = i % 80; wf[i % 12] = *(LAS v4u*)((n < 40 ? W0 : W1) + (n % 40) * 1024); }
        else if constexpr (o.k == OP_XW) { constexpr int k = i >> 1; if constexpr (i < 8) wait_a<o.n>(xa[k]); else wait_a4<o.n>(xa[k], xa[k + 1], xa[k + 2], xa[k + 3]); }
        else if constexpr (o.k == OP_BAR) { if constexpr (i == B_T0) { wait_vm<o.n>(); BAR_RAW(); } else BAR(); }
        else if constexpr (o.k == OP_VM) issue(std::integral_constant<int, i>{});
        else if constexpr (o.k == OP_AND) { sa2[i & 1] = sv[i & 1] & dmk; tie_v(sa2[i & 1]); wait_a<o.n>(wp[i]); }
        else if constexpr (o.k == OP_SMF) mfz(sd[i & 1], sa2[i & 1], wp[i]);
        else if constexpr (o.k == OP_CVT) {
          const f32x16& d = sd[i & 1];
          asm volatile("" ::"v"(d));
          so2[i & 1] = v4u{pk2(d[0], d[1]), pk2(d[2], d[3]), pk2(d[4], d[5]), pk2(d[6], d[7])};
          bias += d[8]; asm volatile("" : "+v"(bias), "+v"(so2[i & 1]));
        } else if constexpr (o.k == OP_WR) { *(LAS v4u*)(wdst[i / 10] + (20 * ((i / 5) & 1) + 2 * (i % 5)) * 1024) = so2[i & 1]; if constexpr (i + 2 < 20) sc_rd(i + 2); }
        else if constexpr (o.k == OP_BMF) { if constexpr (i == 79) { mfv(a0, bf0, ones); mfv(a1, bf1, ones); } else { mfv(c0, bf0, ones); mfv(c1, bf1, ones); } }
        else if constexpr (o.k == OP_MISC) {
          if constexpr (i == 0) {
            const float h = rbf(bias);
            *(LAS unsigned*)(L + L_BIASF + (w & 1) * 512 + (lane & 31) * 16 + ((w & 2) + hh) * 4) = hh ? 0u : pk2(h, bias - h);
          }
          if constexpr (i == 1) { LAS char* bp = L + L_BIASF + (lane & 31) * 16; bf0 = *(LAS v4u*)bp; bf1 = *(LAS v4u*)(bp + 512); trow = LP((int)(size_t)tb + (lane & 31) * TB_ROW + 8 * hh); }
          if constexpr (i == 2) { const unsigned u = hh ? 0u : 0x3F803F80u; ones = v4u{u, u, u, u}; tie_v(ones); }
          if constexpr (i == 3) { *(LAS v2u*)(trow + 128) = v2u{0x3F803F80u, 0x3F803F80u}; tvw = LP((int)(size_t)trow + 80 + 16 * hh); }
          if constexpr (i == 4 || i == 6) { tie_v(mv[i == 6]); pv = msum(mv[i == 6]); asm volatile("" : "+v"(pv)); }
          if constexpr (i == 5) { pv = rbf(pv + ((w & 1) ? 1.f : 0.f)); *(LAS float*)(L + L_PAR + w * 320 + lane * 4) = pv; *(LAS u16*)(L + L_PAR + (w == 3 ? 1536 : 1920) + lane * 2) = (u16)(__float_as_uint(pv) >> 16); }
          if constexpr (i == 7) { pv = rbf(pv + mva); *(LAS float*)(size_t)mvf = pv; *(LAS u16*)(size_t)mvh = (u16)(__float_as_uint(pv) >> 16); }
          if constexpr (i == 8) {
            trd = LP((int)(size_t)tb + (8 * hh + tq) * TB_ROW + (16 * (gam & 1) + 4 * tp) * 2);
            dbase = LP(lbase + L_RWD + (8 * gam + tq) * 256 + 40 * tp);
            cp0 = LP(lbase + L_PAR + 1536 + (lane & 1) * 160 + 16 * hh);
          }
          if constexpr (i == 9) {
            const int g0 = lbase + L_RGU + (gam & 1) * 10240 + (16 * w + 8 * (tp & 1) + 4 * (tp >> 1)) * 2 + (tp == 3 ? ds[15] : 0) + tq * 128;
            gb0 = LP(g0 + hh * 1024); gb1 = LP(g0 + hh * 5120);
          }
        } else if constexpr (o.k == OP_KV) kv_slice<i>(e, a0, a1, trow, tvw, trd, kv0, kv1);
        else if constexpr (o.k == OP_Q) { soft_slice<i, 6>(e.s, a1); if constexpr (i >= 4 && i < 24) asm volatile("" : "+v"(e.s.e[i - 4]), "+v"(e.s.d)); }
        else if constexpr (o.k == OP_TRD) {
          if constexpr (i < 4) { tt[0] = trread(gb0 + 20 * i * 128); tt[1] = trread(gb0 + (20 * i + 4) * 128); tt[2] = *(LAS v2u*)(cp0 + 40 * i); tt[3] = *(LAS v2u*)(cp0 + 40 * i + 8); }
          else if constexpr (i == 4) { tt[0] = trread(gb1 + 16 * 128); tt[1] = trread(gb1 + 36 * 128); tt[2] = *(LAS v2u*)(cp0 + 64 * hh + 32); tt[3] = *(LAS v2u*)(cp0 + 64 * hh + 72); }
          else { tt[0] = trread(dbase + dt[i - 5]); tt[1] = trread(dbase + dt[i - 5] + 1024); }
        } else if constexpr (o.k == OP_TST) {
          if constexpr (i < 8) { tl = v4u{tt[0].x, tt[0].y, tt[1].x, tt[1].y}; stc(rs, v16 | dt[i < 5 ? 4 : i < 7 ? 5 : 6], i < 5 ? ds[8] + i * ds[9] : ds[5 + i], tl); }
          else stc2(rs, (hh == 0 ? lane * 8 : OOB) | dt[4], ds[13], v2u{__float_as_uint(csa[0]), __float_as_uint(csa[1])});
        } else if constexpr (o.k == OP_TMF) { csa = mfma32(v4u{tt[2].x, tt[2].y, tt[3].x, tt[3].y}, tl, csa); asm volatile("" : "+a"(csa)); }
        SCHED();
      });
    }
    Soft q0 = e.s;
    mf_settle(c0, c1);
    sfor<NKV>(SLAM(s) { kv_slice<decltype(s_c)::value>(e, c0, c1, trow, tvw, trd, kv0, kv1); });
    {
      LAS f32x4* kr = (LAS f32x4*)(tb + v16);
#pragma unroll
      for (int g = 0; g < 4; ++g) kr[64 * g] = f32x4{kv0[4 * g], kv0[4 * g + 1], kv0[4 * g + 2], kv0[4 * g + 3]};
      kr[256] = f32x4{kv1[0], kv1[1], kv1[2], kv1[3]};
    }
    BAR();
    {
      LAS f32x4* kr = (LAS f32x4*)(L + L_TB + kvsrc);
      constexpr int Q = TB_SZ / 16;
      const f32x4 s = (kr[0] + kr[Q]) + (kr[2 * Q] + kr[3 * Q]);
      stc(rs, v16, KVP_OFF + (hB * 8 + pB) * 4096 + w * 1024, __builtin_bit_cast(v4u, s));
    }
    wo_run(std::integral_constant<int, 0>{}, std::integral_constant<int, WO_0>{});
    const int sg0 = sig_off(lane < (w == 0 ? 8 : w == 1 ? 16 : (w == 2 && tgC < 28) ? 32 : 0), 0,
                            w == 0 ? C_KV * 256 + hB * 8 + lane : w == 1 ? C_GU * 256 + lane * 16 + fgD : C_WD * 256 + lane * 8 + cgC);
    SCHED();
    sfor<NSM>(SLAM(s) { soft_slice<decltype(s_c)::value, 6>(e.s, c1); });
#pragma unroll
    for (int i = 0; i < 10; ++i) { fq[1][i] = e.s.o[i]; asm volatile("" : "+v"(fq[1][i])); }
    wait_vm<WO_0>();
    BAR();
    signal(rs, sg0);
    {
      const rsrc_t rx = mkrs(x, S_ * D_ * 2);
#pragma unroll
      for (int i = 0; i < 4; ++i) dma_raw(rx, rvo, ((64 * tgC + 16 * w + 4 * i) * 640 + 80 * cgC) * 2, lbase + L_PX + (4 * w + i) * 1024);
    }
    wo_run(std::integral_constant<int, WO_0>{}, std::integral_constant<int, WO_1>{});
    sfor<NSM - BP.qdone>(SLAM(s) { soft_slice<BP.qdone + decltype(s_c)::value, 6>(q0, a1); });
#pragma unroll
    for (int i = 0; i < 10; ++i) { fq[0][i] = q0.o[i]; asm volatile("" : "+v"(fq[0][i])); }
  }
  poll_ge(ws, C_KV, hB * 8 + pB, 8u);
  unsigned pg, pd;
  {
    const int ko = KVP_OFF + hB * 8 * 4096 + w * 1024;
    v4u t[8];
    sfor<8>(SLAM(p) { constexpr int p = decltype(p_c)::value; ald_v<true>(t[p], rs, vv[p % 3], ko + (p / 3) * 12288); });
    wo_run(std::integral_constant<int, WO_1>{}, std::integral_constant<int, WO_2>{});
#pragma unroll
    for (int i = 0; i < 5; ++i) { const v2u q = *(LAS v2u*)(L + L_PX + w * 4096 + cC * 256 + gC * 40 + i * 8); xr[2 * i] = q.x; xr[2 * i + 1] = q.y; }
    wait_v<WO_2 - WO_1 + 4>(t[3]); tie_v(t[0]); tie_v(t[1]); tie_v(t[2]);
    f32x4 s = (__builtin_bit_cast(f32x4, t[0]) + __builtin_bit_cast(f32x4, t[1])) + (__builtin_bit_cast(f32x4, t[2]) + __builtin_bit_cast(f32x4, t[3]));
    asm volatile("" : "+v"(s));
    wait_v<WO_2 - WO_1>(t[7]); tie_v(t[4]); tie_v(t[5]); tie_v(t[6]);
    s += (__builtin_bit_cast(f32x4, t[4]) + __builtin_bit_cast(f32x4, t[5])) + (__builtin_bit_cast(f32x4, t[6]) + __builtin_bit_cast(f32x4, t[7]));
    *(LAS v2u*)(L + L_KVA + kvdst) = v2u{pk2(s.x, s.y), pk2(s.z, s.w)};
    BAR();
    ald_u(pg, rs, CNT_OFF + (C_GU * 256 + tgD * 16 + fgD) * 256);
    ald_u(pd, rs, CNT_OFF + (C_WD * 256 + tgC * 8 + cgC) * 256);
    const v2u A2l = *(LAS v2u*)(L + L_KVA + 2048 + v16);
    const v4u A0 = *(LAS v4u*)(L + L_KVA + v16), A1 = *(LAS v4u*)(L + L_KVA + 1024 + v16), A2 = {A2l.x, A2l.y, 0u, 0u};
    const int vo = ((lane >> 4) & 1) * 20480 + (lane & 15) * 16, vt = ((lane >> 4) & 1) * 20480 + (lane & 15) * 8;
    f32x16 o[2];
#pragma unroll
    for (int T = 0; T < 2; ++T) {
      mfzv(o[T], A0, v4u{fq[T][0], fq[T][1], fq[T][2], fq[T][3]});
      mfv(o[T], A1, v4u{fq[T][4], fq[T][5], fq[T][6], fq[T][7]});
      mfv(o[T], A2, v4u{fq[T][8], fq[T][9], 0u, 0u});
    }
#pragma unroll
    for (int T = 0; T < 2; ++T) {
      if (T == 0) asm volatile("" : "+v"(o[0])); else mf_settle1(o[1]);
      const float r = __builtin_amdgcn_rcpf(o[T][12] + 1e-6f);
      const int t16 = 16 * pB + 4 * w + 2 * (T ^ flip);
      stc(rs, vo + hh * 256, OT_OFF + t16 * 20480 + (hB >> 1) * 1024 + (hB & 1) * 512,
          v4u{pk2(o[T][0] * r, o[T][1] * r), pk2(o[T][2] * r, o[T][3] * r), pk2(o[T][4] * r, o[T][5] * r), pk2(o[T][6] * r, o[T][7] * r)});
      stc2(rs, hh == 0 ? vt : OOB, OT_OFF + t16 * 20480 + 16384 + hB * 128,
           v2u{pk2(o[T][8] * r, o[T][9] * r), pk2(o[T][10] * r, o[T][11] * r)});
    }
  }
  wo_run(std::integral_constant<int, WO_2>{}, std::integral_constant<int, 25>{});
  const int sg1 = sig_off(tid < 32, C_O, pB * 32 + lane);
  wait_vm<25 - WO_2>();
  BAR_RAW();
  signal(rs, sg1);

  const int wdo = WD_OFF + cgC * 286720 + w * 71680;
  v4u wdp[34], wl[18];
  auto ldl = [&](auto j_c) __attribute__((always_inline)) { constexpr int j = decltype(j_c)::value; ald_v<true, (j & 3) * 1024>(wl[j], rs, vv[(j % 12) >> 2], wdo + (52 + j / 12 * 12) * 1024); };
  auto ldw = [&](auto n_c) __attribute__((always_inline)) {
    constexpr int n = decltype(n_c)::value;
    if constexpr (n < 22) ald_a<true, (n & 3) * 1024>(wdp[n], rs, vv[(n % 12) >> 2], wdo + (18 + n / 12 * 12) * 1024);
    else ald_v<true, (n & 3) * 1024>(wdp[n], rs, vv[(n % 12) >> 2], wdo + (18 + n / 12 * 12) * 1024);
  };
  const int r6 = cgC >= 6 ? cgC - 6 : cgC, t32 = 4 * tgD + w;
  int kso[14], xb8[8];
  const int sg2 = sig_off(tid < 16, C_X, (tgC >> 1) * 16 + lane), sg3 = sig_off(tid < 16, C_A1, tgD * 16 + lane);
  unsigned x1p[10];
  {
    wait_u<63>(pg); wait_u<63>(pd);
    if (__builtin_expect(__builtin_amdgcn_readfirstlane(pg) < 16u, 0)) poll_ge(ws, C_GU, tgD * 16 + fgD, 16u);
    if (__builtin_expect(__builtin_amdgcn_readfirstlane(pd) < 28u, 0)) poll_ge(ws, C_WD, tgC * 8 + cgC, 28u);
    {
      v4u t[8];
      sfor<8>(SLAM(p) { constexpr int p = decltype(p_c)::value; ald_v<true, (p & 1) * 1792>(t[p], rs, tid < 112 ? tid * 16 : OOB, CSB_OFF + (fgD * 8 + (p & 6)) * 1792); });
      sfor<WP_2>(ldw);
      wait_v<WP_2>(t[7]);
      f32x4 s = ZERO4;
      sfor<8>(SLAM(p) { constexpr int p = decltype(p_c)::value; tie_v(t[p]); s += __builtin_bit_cast(f32x4, t[p]); });
      float h[4], m[4], l[4];
#pragma unroll
      for (int i = 0; i < 4; ++i) { h[i] = rbf(s[i]); m[i] = rbf(s[i] - h[i]); l[i] = (s[i] - h[i]) - m[i]; }
      const int ca = min(tid, 112) * 32;
      *(LAS v4u*)(L + L_CSF + ca) = v4u{pk2(h[0], m[0]), pk2(l[0], h[1]), pk2(m[1], l[1]), 0u};
      *(LAS v4u*)(L + L_CSF + ca + 16) = v4u{pk2(h[2], m[2]), pk2(l[2], h[3]), pk2(m[3], l[3]), 0u};
    }
#pragma unroll
    for (int s = 0; s < 14; ++s) {
      const int q1 = s + r6;
      const int ks = s < 6 ? 6 * w + (q1 >= 6 ? q1 - 6 : q1) : (s < 10 ? 24 : 40) + 4 * w + ((s - 6 + cgC) & 3);
      kso[s] = ACT_OFF + 4 * tgC * 57344 + ks * 1024; asm volatile("" : "+s"(kso[s]));
    }
#pragma unroll
    for (int i = 0; i < 8; ++i) { xb8[i] = XS_OFF + t32 * 40960 + ((i + fgD) & 7) * 5120; asm volatile("" : "+s"(xb8[i])); }
    float xin[20];
#pragma unroll
    for (int i = 0; i < 10; ++i) { xin[2 * i] = bflo(xr[i]); xin[2 * i + 1] = bfhi(xr[i]); }
#pragma unroll
    for (int i = 0; i < 20; ++i) asm volatile("" : "+v"(xin[i]));
    int ob[9];
#pragma unroll
    for (int i = 0; i < 9; ++i) { ob[i] = OT_OFF + (4 * tgC + w) * 20480 + (i < 8 ? (i + cgC) & 7 : 8) * 2048; asm volatile("" : "+s"(ob[i])); }
    const int otv = opaque(gC * 256 + cC * 8);
    const int stv = opaque(gC == 0 ? cC * 8 : OOB), xv0 = opaque(gC * 1024 + cC * 16), xv1 = opaque((gC >> 1) * 512 + cC * 16 + (gC & 1) * 8);
    f32x4 g4[5], c4[5];
#pragma unroll
    for (int ct = 0; ct < 5; ++ct) { g4[ct] = *(LAS f32x4*)(L + L_PAR + gC * 80 + ct * 16); c4[ct] = *(LAS f32x4*)(L + L_PAR + 320 + gC * 80 + ct * 16); asm volatile("" : "+v"(g4[ct]), "+v"(c4[ct])); }
    poll_ge(ws, C_O, tgC * 8 + cgC, 32u);
    v4u of[16];
    v2u ot[8];
    auto ldo = [&](auto k_c) __attribute__((always_inline)) {
      constexpr int k = decltype(k_c)::value;
      if constexpr (k < 16) ald_a<true, (k & 1) * 1024>(of[k], rs, v16, ob[k >> 1]);
      else ald2_v<((k - 16) >> 1) * 1024 + (k & 1) * 128>(ot[k - 16], rs, otv, ob[8]);
    };
    sfor<12>(ldo);
    BAR_RAW();
    f32x4 acc[5] = {ZERO4, ZERO4, ZERO4, ZERO4, ZERO4};
    LAS char* WA = (LAS char*)(size_t)opaque(lbase + v16 + 3 * SLOT);
    LAS char* WB = L + v16;
    auto wrd = [&](int n) __attribute__((always_inline)) { return n < 36 ? *(LAS v4u*)(WA + n * 1024) : *(LAS v4u*)(WB + (n - 36) * 1024); };
    v4u wf[3][5];
#pragma unroll
    for (int ct = 0; ct < 10; ++ct) wf[ct / 5][ct % 5] = wrd(ct);
    sfor<20>(SLAM(k) {
      constexpr int k = decltype(k_c)::value;
      if constexpr (k < 6) { ldo(std::integral_constant<int, 12 + 2 * k>{}); ldo(std::integral_constant<int, 13 + 2 * k>{}); }
      constexpr int NS = 9, NX = NS + WP_C;
      static_assert(NX <= 14, "");
      if constexpr (k >= 6 && k < 6 + NS) dmap<true, k - 6>(rs, v16, guo + 9 * w * 1024, lbase + 2 * SLOT + 9 * w * 1024);
      if constexpr (k >= 6 + NS && k < 6 + NX) ldw(std::integral_constant<int, WP_2 + k - 6 - NS>{});
      constexpr int done = k < 6 ? 14 + 2 * k : k < 6 + NX ? 19 + k : 24 + NX;
      v4u bo;
      if constexpr (k < 16) { wait_a<done - 1 - k>(of[k]); bo = of[k]; }
      else { wait2_v<done - 1 - (2 * k - 15)>(ot[2 * k - 32], ot[2 * k - 31]); bo = v4u{ot[2 * k - 32].x, ot[2 * k - 32].y, ot[2 * k - 31].x, ot[2 * k - 31].y}; }
      need(wf[k % 3][4]);
#pragma unroll
      for (int ct = 0; ct < 5; ++ct) {
        acc[ct] = mfma16(wf[k % 3][ct], bo, acc[ct]);
        if constexpr (k + 2 < 20) wf[(k + 2) % 3][ct] = wrd((k + 2) * 5 + ct);
      }
      SCHED();
    });
    unsigned xs[10];
    f32x2 sm = {0.f, 0.f}, sq = {0.f, 0.f};
#pragma unroll
    for (int ct = 0; ct < 5; ++ct) {
      x1p[2 * ct] = pk2(fmaf(g4[ct].x, acc[ct].x, xin[4 * ct]), fmaf(g4[ct].y, acc[ct].y, xin[4 * ct + 1]));
      x1p[2 * ct + 1] = pk2(fmaf(g4[ct].z, acc[ct].z, xin[4 * ct + 2]), fmaf(g4[ct].w, acc[ct].w, xin[4 * ct + 3]));
      const f32x2 ya = {bflo(x1p[2 * ct]), bfhi(x1p[2 * ct])}, yb = {bflo(x1p[2 * ct + 1]), bfhi(x1p[2 * ct + 1])};
      xs[2 * ct] = pk2(ya.x * c4[ct].x, ya.y * c4[ct].y);
      xs[2 * ct + 1] = pk2(yb.x * c4[ct].z, yb.y * c4[ct].w);
      sm += ya + yb; sq += ya * ya; sq += yb * yb;
    }
    const float sum = swapadd(swap16add(sm.x + sm.y)), ssq = swapadd(swap16add(sq.x + sq.y));
    stc2(rs, stv, ST_OFF + (cgC * 2048 + 64 * tgC + 16 * w) * 8, v2u{__float_as_uint(sum), __float_as_uint(fmaxf(ssq - sum * sum * (1.f / 80.f), 0.f))});
    const int so = XS_OFF + (2 * tgC + (w >> 1)) * 40960 + 5 * cgC * 1024 + (w & 1) * 256;
    stc(rs, xv0, so, v4u{xs[0], xs[1], xs[2], xs[3]});
    stc(rs, xv0, so + 512, v4u{xs[4], xs[5], xs[6], xs[7]});
    stc2(rs, xv1, so + 4096, v2u{xs[8], xs[9]});
  }
  BAR_RAW();
  dmaN<true, 9>(rs, v16, guo + (36 + 9 * w) * 1024, lbase + 3 * SLOT + 9 * w * 1024);
  wait_vm<9>();
  BAR_RAW();
  signal(rs, sg2);

  v4u af[56];
  int tto[4];
#pragma unroll
  for (int u = 0; u < 4; ++u) tto[u] = opaque(v16 + ((w + u) & 3) * 57344);
  const int pwo = __builtin_amdgcn_readfirstlane(CNT_OFF + C_A1 * 65536 + (tgC * 8 + cgC) * 256);
  auto lda1 = [&](auto g_c) __attribute__((always_inline)) { constexpr int g = decltype(g_c)::value; ald_a<true>(af[g], rs, tto[g & 3], kso[g >> 2]); };
  {
    dmaN<true, 9>(rs, v16, guo + (72 + 9 * w) * 1024, lbase + 9 * w * 1024);
    sfor<WP_3>(SLAM(j) { ldw(std::integral_constant<int, WP_2 + WP_C + decltype(j_c)::value>{}); });
    poll_ge(ws, C_X, tgD * 16 + fgD, 16u);
    v4u xf[40], xc, cf;
    v2u st[4];
    auto ldx = [&](auto k_c) __attribute__((always_inline)) { constexpr int k = decltype(k_c)::value, r = k % 5; ald_a<true, (r & 3) * 1024>(xf[k], rs, vv[r >> 2], xb8[k / 5]); };
    sfor<D_XP>(ldx);
    sfor<4>(SLAM(u) { constexpr int u = decltype(u_c)::value; ald2_v<0>(st[u], rs, (lane & 31) * 8 + hh * 65536, ST_OFF + t32 * 256 + u * 16384); });
    BAR();
    float k1, k3, inv, mu, sd;
    f32x16 acc[7];
    constexpr int PD = 8, NW = 12;
    v4u wf[NW];
    LAS char* SL[4];
#pragma unroll
    for (int s = 0; s < 4; ++s) SL[s] = (LAS char*)(size_t)opaque(lbase + v16 + s * SLOT);
    auto wrd = [&](int i) __attribute__((always_inline)) { return *(LAS v4u*)(SL[(i / 36 + 2) & 3] + (i % 36) * 1024); };
#pragma unroll
    for (int i = 0; i < PD; ++i) wf[i] = wrd(i);
    const int ao = ACT_OFF + 2 * t32 * 57344;
    const int avo = ((lane >> 4) & 1) * 57344 + (lane & 15) * 16;
    const int avh = avo + hh * 256;
    float ta, tb, na, nb, pa, pb;
    unsigned pw, aq[4];
    auto ge = [&](const f32x16& a, int et, int s) __attribute__((always_inline)) {
      if (s == 0) { na = vmul(a[0], a[0]); nb = vmul(a[1], a[1]); pa = vmul(a[0], a[8]); pb = vmul(a[1], a[9]); return; }
      if (s == NES - 1) { stc(rs, avh, ao + (et < 3 ? 6 * fgD + 2 * et : et < 5 ? 96 + 4 * fgD + 2 * et - 6 : 160 + 4 * fgD) * 256, v4u{aq[0], aq[1], aq[2], aq[3]}); return; }
      const int q = (s - 1) / 5, z = (s - 1) % 5;
      if (z == 0) { ta = fmaf(na, k3, k1); asm volatile("" : "+v"(ta)); tb = fmaf(nb, k3, k1); ta = vmul(ta, a[2 * q]); tb = vmul(tb, a[2 * q + 1]); }
      else if (z == 1) { ta = __builtin_amdgcn_exp2f(ta); tb = __builtin_amdgcn_exp2f(tb); }
      else if (z == 2) { ta = fmaf(ta, inv, inv); asm volatile("" : "+v"(ta)); tb = fmaf(tb, inv, inv); if (q < 3) { na = vmul(a[2 * q + 2], a[2 * q + 2]); nb = vmul(a[2 * q + 3], a[2 * q + 3]); } }
      else if (z == 3) { ta = __builtin_amdgcn_rcpf(ta); tb = __builtin_amdgcn_rcpf(tb); }
      else { aq[q] = pk2(vmul(pa, ta), vmul(pb, tb)); if (q < 3) { pa = vmul(a[2 * q + 2], a[2 * q + 10]); pb = vmul(a[2 * q + 3], a[2 * q + 11]); } }
    };
    auto F = [](unsigned u) { return __uint_as_float(u); };
    LAS char* csf = L + L_CSF + (lane & 31) * 16;
    sfor<280>(SLAM(i) {
      constexpr int i = decltype(i_c)::value;
      if constexpr (i % 36 == 28 && i > 36 && i < 240) {
        constexpr int s = i / 36;
        if constexpr (s >= 2 && s <= 4) wait_vm<cap63(DS.cnt[i - 1] - DS.cnt[d_dstep(s - 2, 8)])>();
        if constexpr (s == 5) wait_vm<DS.cnt[i - 1] - DS.cnt[DS.st[2]]>();
        BAR_RAW();
        if constexpr (s == 5) signal(rs, sg3);
      }
      if constexpr (i == 226) { wait_vm<DS.cnt[i - 1] - DS.cnt[d_dstep(4, 6)]>(); BAR_RAW(); }
      if constexpr (i == D_P2) { wait_vm<DS.cnt[i - 1] - DS.cnt[DS.st[4]]>(); BAR_RAW(); signal(rs, sg3 + 65536); }
      constexpr int ft = i < 120 ? i % 3 : 3 + (i - 120) / 40, k = i < 120 ? i / 3 : (i - 120) % 40;
      constexpr int done = i == 0 ? D_XP + 4 : DS.cnt[i - 1];
      if constexpr (i == 0 || i == 3) wait_a<done - 1 - d_xidx(k)>(xf[k]);
      if constexpr (i >= 6 && i < 120 && i % 6 == 0) wait_a2<done - 1 - d_xidx(k + 1)>(xf[k], xf[k + 1]);
      if constexpr (i % 4 == 0) need(wf[(i + 3) % NW]);
      mfa<k == 0>(acc[ft], wf[i % NW], xf[k]);
      SCHED();
      if constexpr (i + PD < 280) wf[(i + PD) % NW] = wrd(i + PD);
      if constexpr (DS.x[i] >= 0) ldx(std::integral_constant<int, DS.x[i]>{});
      if constexpr (DS.d[i] >= 0) {
        constexpr int g = DS.d[i] / 9, j = DS.d[i] % 9;
        constexpr int per = g == 4 ? 7 : 9;
        dmap<true, j>(rs, v16, g < 5 ? guo + ((g + 3) * 36 + w * per) * 1024 : wdo + (g - 5) * 9216, lbase + ((g + 1) & 3) * SLOT + w * per * 1024);
      }
      if constexpr (DS.p[i] >= 34) ldl(std::integral_constant<int, DS.p[i] - 34>{});
      else if constexpr (DS.p[i] >= 0) ldw(std::integral_constant<int, DS.p[i]>{});
      if constexpr (i == D_PL + 1) ald_u(pw, rs, pwo);
      if constexpr (i == D_PC) {
        wait_u<DS.cnt[D_PC] - DS.cnt[D_PL + 1]>(pw);
        if (__builtin_expect(__builtin_amdgcn_readfirstlane(pw) < 16u, 0)) poll_ge(ws, C_A1, tgC * 8 + cgC, 16u);
        CFENCE();
      }
      if constexpr (DS.a[i] >= 0) lda1(std::integral_constant<int, DS.a[i]>{});
      SCHED();
      if constexpr (i == 59) {
        wait2_v<63>(st[0], st[1]); wait2_v<63>(st[2], st[3]);
        mu = (F(st[0].x) + F(st[1].x)) + (F(st[2].x) + F(st[3].x)); sd = (F(st[0].y) + F(st[1].y)) + (F(st[2].y) + F(st[3].y));
      }
      if constexpr (i == 61) mu = swapadd(mu) * (1.f / 640.f);
      if constexpr (i == 63) { ta = fmaf(F(st[0].x), 1.f / 80.f, -mu); tb = fmaf(F(st[1].x), 1.f / 80.f, -mu); ta = vmul(ta, ta); ta = fmaf(tb, tb, ta); }
      if constexpr (i == 65) { na = fmaf(F(st[2].x), 1.f / 80.f, -mu); nb = fmaf(F(st[3].x), 1.f / 80.f, -mu); ta = fmaf(na, na, ta); ta = fmaf(nb, nb, ta); }
      if constexpr (i == 67) sd = swapadd(fmaf(ta, 80.f, sd));
      if constexpr (i == 69) sd = rbf(rbf(sd * (1.f / 640.f)) + 1e-5f);
      if constexpr (i == 73) sd = rbf(__builtin_amdgcn_sqrtf(sd));
      if constexpr (i == 75) { ta = __builtin_amdgcn_rcpf(sd); inv = sd * sd; }
      if constexpr (i == 77) { k1 = -2.3022082f * ta; k3 = -0.10294324f * ta * ta * ta; }
      if constexpr (i == 79) {
        const unsigned a = pk2(-mu, -mu), c = pk2(sd, sd);
        xc = v4u{a, (a & 0xffffu) | (c & 0xffff0000u), c, 0u};
      }
      if constexpr (i == 81) xc = hh ? v4u{0u, 0u, 0u, 0u} : xc;
      if constexpr (i >= 86 && i <= 102 && i % 8 == 6) cf = *(LAS v4u*)(csf + ((i - 86) / 8) * 512);
      if constexpr (i >= 92 && i <= 108 && i % 8 == 4) mfv(acc[(i - 92) / 8], cf, xc);
      if constexpr (i >= 120 && k == 22) cf = *(LAS v4u*)(csf + ft * 512);
      if constexpr (i >= 120 && k == 28) mfv(acc[ft], cf, xc);
      if constexpr (DS.e[i] >= 0) ge(acc[DS.e[i] / NES], DS.e[i] / NES, DS.e[i] % NES);
      SCHED();
    });
    mf_settle1(acc[6]);
    {
      float av[8];
#pragma unroll
      for (int v = 0; v < 8; ++v) {
        const float g = acc[6][v];
        av[v] = g * acc[6][8 + v] * __builtin_amdgcn_rcpf(fmaf(__builtin_amdgcn_exp2f(fmaf(g * g, k3, k1) * g), inv, inv));
      }
      stc(rs, avh, ao + (160 + 4 * fgD + 2) * 256, v4u{pk2(av[0], av[1]), pk2(av[2], av[3]), pk2(av[4], av[5]), pk2(av[6], av[7])});
    }
  }

  {
    unsigned p2, p3;
    auto eop = [&](auto n_c) __attribute__((always_inline)) {
      constexpr int o = ES.op[decltype(n_c)::value];
      if constexpr (o < 100) lda1(std::integral_constant<int, o>{});
      else if constexpr (o < 200) ldl(std::integral_constant<int, o - 100>{});
      else if constexpr (o == 200) ald_u(p2, rs, pwo + 65536);
      else ald_u(p3, rs, pwo + 131072);
    };
    sfor<ES.cnt[0]>(eop);
    LAS char* mine2 = (LAS char*)(size_t)opaque(lbase + w * 9 * 1024 + v16 + 2 * SLOT);
    wait_vm<ES.cnt[0] + 1 + D_NA - 4>();
    sfor<34>(SLAM(n) { constexpr int n = decltype(n_c)::value; if constexpr (n < 22) tie_a(wdp[n]); else tie_v(wdp[n]); });
    sfor<WL_D>(SLAM(j) { tie_v(wl[decltype(j_c)::value]); });
    v4u wc[18];
#pragma unroll
    for (int n = 0; n < 10; ++n) wc[n] = *(LAS v4u*)(mine2 + (n / 9) * SLOT + (n % 9) * 1024);
    f32x4 acc[4][5];
    auto grp = [&](auto g_c, auto q_c) __attribute__((always_inline)) {
      constexpr int g = decltype(g_c)::value, q = decltype(q_c)::value, s = g >> 2, u = g & 3;
      if constexpr (q == E_P3) { wait_vm<ES.cnt[q]>(); BAR_RAW(); signal(rs, sg3 + 131072); }
      if constexpr (q == E_C2 || q == E_C3) {
        unsigned& p = q == E_C2 ? p2 : p3;
        wait_u<ES.cnt[q] - 1 - (q == E_C2 ? ES.p2 : ES.p3)>(p);
        if (__builtin_expect(__builtin_amdgcn_readfirstlane(p) < 16u, 0)) poll_ge(ws, q == E_C2 ? C_A2 : C_A3, tgC * 8 + cgC, 16u);
        CFENCE();
      }
      if constexpr (g < D_NA) wait_a<cap63(ES.cnt[q] + D_NA - g)>(af[g]);
      else wait_a<cap63(ES.cnt[q] - 1 - ES.aidx[g])>(af[g]);
      sfor<5>(SLAM(ct) {
        constexpr int ct = decltype(ct_c)::value, n = 5 * s + ct;
        if constexpr (n >= 52 + WL_D && (u == 0 || q >= 48)) wait_v<cap63(ES.cnt[q] - 1 - ES.widx[n - 52])>(wl[n - 52]);
        const v4u& a = n < 18 ? wc[n < 18 ? n : 0] : n < 52 ? wdp[(n >= 18 && n < 52) ? n - 18 : 0] : wl[n >= 52 ? n - 52 : 0];
        acc[u][ct] = mfma16(a, af[g], s == 0 ? ZERO4 : acc[u][ct]);
        if constexpr (ct == 0) { SCHED(); sfor<ES.cnt[q + 1] - ES.cnt[q]>(SLAM(j) { eop(std::integral_constant<int, ES.cnt[q] + decltype(j_c)::value>{}); }); SCHED(); }
        if constexpr (q < 8 && ct == 2) wc[10 + q] = *(LAS v4u*)(mine2 + SLOT + (1 + q) * 1024);
        if constexpr (q >= 50 && ct < 3) {
          constexpr int uu = 3 - (q - 50) / 2, c2 = (q & 1) * 3 + ct;
          if constexpr (c2 < 5) { constexpr int f = (uu - 1) * 5 + c2; *(LAS f32x4*)(mine2 + (f >> 3) * SLOT + (f & 7) * 1024) = acc[uu][c2]; }
        }
      });
      SCHED();
    };
    sfor<48>(SLAM(q) { grp(q_c, q_c); });
    sfor<8>(SLAM(r) { constexpr int r = decltype(r_c)::value; grp(std::integral_constant<int, 48 + 4 * (r & 1) + 3 - (r >> 1)>{}, std::integral_constant<int, 48 + r>{}); });
    BAR();
    unsigned o[10];
#pragma unroll
    for (int ct = 0; ct < 5; ++ct) {
      f32x4 s = acc[0][ct];
#pragma unroll
      for (int u = 1; u < 4; ++u) { const int f = (u - 1) * 5 + ct; s += *(LAS f32x4*)(L + 2 * SLOT + ((w - u) & 3) * 9 * 1024 + v16 + (f >> 3) * SLOT + (f & 7) * 1024); }
      const f32x4 g4 = *(LAS f32x4*)(L + L_PAR + 640 + gC * 80 + ct * 16);
      o[2 * ct] = pk2(fmaf(g4.x, s.x, bflo(x1p[2 * ct])), fmaf(g4.y, s.y, bfhi(x1p[2 * ct])));
      o[2 * ct + 1] = pk2(fmaf(g4.z, s.z, bflo(x1p[2 * ct + 1])), fmaf(g4.w, s.w, bfhi(x1p[2 * ct + 1])));
    }
    LAS char* ow = L + w * 9216;
#pragma unroll
    for (int i = 0; i < 5; ++i) *(LAS v2u*)(ow + cC * 176 + gC * 40 + i * 8) = v2u{o[2 * i], o[2 * i + 1]};
    CFENCE();
    const rsrc_t ro = mkrs(out, S_ * D_ * 2);
#pragma unroll
    for (int i = 0; i < 4; ++i)
      __builtin_amdgcn_raw_buffer_store_b128(*(LAS v4u*)(ow + (4 * i + gC) * 176 + min(cC, 9) * 16), ro, rvo + ((64 * tgC + 16 * w + 4 * i) * 640 + 80 * cgC) * 2, 0, 0);
  }
  asm volatile(".Lkm_end:");
}

extern "C" void kernel_launch(void* const* d_in, const int* in_sizes, int n_in, void* d_out, int out_size, void* d_ws, size_t ws_size,
                              hipStream_t stream) {
  (void)in_sizes; (void)n_in; (void)out_size; (void)ws_size;
  (void)hipFuncSetAttribute((const void*)k_main, hipFuncAttributeMaxDynamicSharedMemorySize, MAIN_LDS);
  k_prep<<<K1_GRID, 256, 0, stream>>>((const u16*)d_in[0], (const u16*)d_in[1], (const u16*)d_in[2], (const u16*)d_in[3], (const u16*)d_in[8],
                                      (const u16*)d_in[9], (char*)d_ws);
  k_main<<<256, 256, MAIN_LDS, stream>>>((const u16*)d_in[0], (const u16*)d_in[5], (const u16*)d_in[6], (const u16*)d_in[7], (char*)d_ws, (u16*)d_out);
}
```
